```python
import torch

def _softmax(x, axis=-1):
    return torch.softmax(x, dim=axis)
def _gelu(x):
    return torch.nn.functional.gelu(x, approximate="tanh")
def _silu(x):
    return torch.nn.functional.silu(x)
def _sigmoid(x):
    return torch.sigmoid(x)
def _layernorm(x, axis=-1, eps=1e-5):
    m = x.mean(dim=axis, keepdim=True)
    v = x.var(dim=axis, keepdim=True, unbiased=False)
    return (x - m) / torch.sqrt(v + eps)
def _scatter_add(tgt, idx, src, axis=0):
    return tgt.clone().index_add_(axis, idx, src)
def _segment_sum(src, seg, n):
    out = torch.zeros((n,) + src.shape[1:], dtype=src.dtype)
    return out.index_add_(0, seg, src)
def _pad(x, pw):
    flat = tuple(v for pair in reversed(pw) for v in pair)
    return torch.nn.functional.pad(x, flat)

kernel_name = 'gated_mix__mha_block_cached__moe_arch_mod__decode'

def setup_inputs():

    _w = (lambda a: (lambda t: t.to(torch.bfloat16) if t.is_floating_point() else t)(torch.as_tensor(a)))
    def _A_setup():
        B, Sq, D = 8, 1, 512
        H, Sk, Dh = 32, 4096, 128
        return {
            "x": _w((lambda *s: torch.randn(*s, dtype=torch.bfloat16) * 0.05)(B, Sq, D)),
            "k_cache": _w((lambda *s: torch.randn(*s, dtype=torch.bfloat16) * 3.2)(B, H, Sk, Dh)),
            "v_cache": _w((lambda *s: torch.randn(*s, dtype=torch.bfloat16) * 1.6)(B, H, Sk, Dh)),
            "wq": _w((lambda *s: torch.randn(*s, dtype=torch.bfloat16) * 3.2)(D, H * Dh) * (D ** -0.5)),
            "wo": _w((lambda *s: torch.randn(*s, dtype=torch.bfloat16) * 1.6)(H * Dh, D) * ((H * Dh) ** -0.5)),
        }
    _A_in = _A_setup()

    def _B_setup():
        B, S, D, F, C = 8, 1, 512, 1792, 1
        return {
            "x": _w((lambda *s: torch.randn(*s, dtype=torch.bfloat16) * 0.05)(B, S, D)),
            "w_r": _w((lambda *s: torch.randn(*s, dtype=torch.bfloat16) * 1.6)(D)),
            "w_gate": _w((lambda *s: torch.randn(*s, dtype=torch.bfloat16) * 0.1)(D, F)),
            "w_up": _w((lambda *s: torch.randn(*s, dtype=torch.bfloat16) * 0.1)(D, F)),
            "w_down": _w((lambda *s: torch.randn(*s, dtype=torch.bfloat16) * 0.1)(F, D)),
        }
    _B_in = _B_setup()

    return {
        "A_x": _A_in["x"],
        "A_k_cache": _A_in["k_cache"],
        "A_v_cache": _A_in["v_cache"],
        "A_wq": _A_in["wq"],
        "A_wo": _A_in["wo"],
        "B_x": _B_in["x"],
        "B_w_r": _B_in["w_r"],
        "B_w_gate": _B_in["w_gate"],
        "B_w_up": _B_in["w_up"],
        "B_w_down": _B_in["w_down"],
        "gate_w": _w((lambda *s: torch.randn(*s, dtype=torch.bfloat16) * 0.05)(512) * 16.0),
    }

def reference(A_x, A_k_cache, A_v_cache, A_wq, A_wo, B_x, B_w_r, B_w_gate, B_w_up, B_w_down, gate_w):
    def _A_ref(x, k_cache, v_cache, wq, wo):
        B, Sq, D = x.shape
        H, Dh = 32, 128
        q = (lambda x, *d: x.permute(*d))((x @ wq).reshape(B, Sq, H, Dh), 0, 2, 1, 3)
        scores = torch.einsum("bhqd,bhkd->bhqk", q, k_cache) * (Dh ** -0.5)
        p = _softmax(scores, axis=-1)
        o = torch.einsum("bhqk,bhkd->bhqd", p, v_cache)
        return (lambda x, *d: x.permute(*d))(o, 0, 2, 1, 3).reshape(B, Sq, H * Dh) @ wo

    def _B_ref(x, w_r, w_gate, w_up, w_down):
        B, S, D = x.shape
        C = 1
        r = x @ w_r
        idx = torch.argsort(-r, axis=-1)[:, :C]
        idx_e = (lambda x, n, axis: torch.repeat_interleave(x, n, dim=axis))(idx.reshape(B, C, 1), D, axis=2)
        x_sel = (lambda x, i, axis: torch.take_along_dim(x, i, dim=axis))(x, idx_e, axis=1)
        g = _silu(x_sel @ w_gate)
        u = x_sel @ w_up
        y = (g * u) @ w_down
        r_sel = _sigmoid((lambda x, i, axis: torch.take_along_dim(x, i, dim=axis))(r, idx, axis=1))
        y = y * r_sel.reshape(B, C, 1)
        b_off = (torch.arange(B) * S).reshape(B, 1)
        flat_idx = (idx + b_off).reshape(B * C)
        zero = (x * 0.0).reshape(B * S, D)
        upd = _scatter_add(zero, flat_idx, y.reshape(B * C, D), axis=0)
        return x + upd.reshape(B, S, D)

    x = A_x
    g = _sigmoid((x * gate_w).sum(-1, keepdims=True))
    a = _A_ref(x, A_k_cache, A_v_cache, A_wq, A_wo)
    b = _B_ref(x, B_w_r, B_w_gate, B_w_up, B_w_down)
    return g * a + (1 - g) * b
```

```cpp
#include <hip/hip_runtime.h>
#include <hip/hip_bf16.h>

typedef unsigned u32x4 __attribute__((ext_vector_type(4)));
typedef unsigned u32x2 __attribute__((ext_vector_type(2)));
typedef float f32x4 __attribute__((ext_vector_type(4)));
typedef __bf16 bf16x2 __attribute__((ext_vector_type(2)));
typedef __bf16 bf16x8 __attribute__((ext_vector_type(8)));
#define GAS __attribute__((address_space(1)))
typedef GAS unsigned gu32;

constexpr int NB = 8, NH = 32, SK = 4096, DH = 128, DM = 512, FF = 1792;
constexpr int NSW = 4;
constexpr int NT = 512;
constexpr int TILES = SK / 16 / NSW;
#define BURST 3
constexpr float C2 = 0.08838834764831845f * 1.4426950408889634f;
constexpr int RSRC3 = 0x00020000;
constexpr int AUX_NT = 2, AUX_SC1 = 16;
constexpr int WS_EPOCH = 0, WS_INBOX = 4096;
constexpr unsigned SPIN_CAP = 1u << 16;

struct Params {
  const unsigned short *xA, *K, *V, *wq, *wo, *xB, *wr, *wg, *wu, *wd, *gw;
  unsigned short* out;
  unsigned char* ws;
};

constexpr int L_QP = 0;
constexpr int L_MRG = L_QP + 512;
constexpr int L_Y = L_MRG + 4 * 132;
constexpr int L_H = L_Y + 512;
constexpr int L_SC = L_H + 64;
constexpr int L_END = L_SC + 8;

__device__ __forceinline__ float dot2(unsigned a, unsigned b, float c) {
  return __builtin_amdgcn_fdot2_f32_bf16(__builtin_bit_cast(bf16x2, a), __builtin_bit_cast(bf16x2, b), c, false);
}
__device__ __forceinline__ unsigned pk_bf16(float lo, float hi) {
  bf16x2 v = {(__bf16)lo, (__bf16)hi};
  return __builtin_bit_cast(unsigned, v);
}
__device__ __forceinline__ float rnd_bf16(float x) { return (float)(__bf16)x; }
__device__ __forceinline__ float bf_lo(unsigned u) { return __builtin_bit_cast(float, u << 16); }
__device__ __forceinline__ float bf_hi(unsigned u) { return __builtin_bit_cast(float, u & 0xffff0000u); }
template <int CTRL> __device__ __forceinline__ float dppf(float x) {
  return __builtin_bit_cast(float, __builtin_amdgcn_mov_dpp(__builtin_bit_cast(int, x), CTRL, 0xf, 0xf, true));
}
template <int CTRL> __device__ __forceinline__ unsigned dppu(unsigned x) {
  return (unsigned)__builtin_amdgcn_mov_dpp((int)x, CTRL, 0xf, 0xf, true);
}
constexpr int XOR1 = 0xB1, XOR2 = 0x4E, XOR7 = 0x141, ROR8 = 0x128, ROR4 = 0x124, BCAST = 0x150;
__device__ __forceinline__ float xrow_sum(float x) {
  auto s = __builtin_amdgcn_permlane16_swap(__float_as_uint(x), __float_as_uint(x), false, false);
  x = __uint_as_float(s[0]) + __uint_as_float(s[1]);
  auto t = __builtin_amdgcn_permlane32_swap(__float_as_uint(x), __float_as_uint(x), false, false);
  return __uint_as_float(t[0]) + __uint_as_float(t[1]);
}
__device__ __forceinline__ float xrow_max(float x) {
  auto s = __builtin_amdgcn_permlane16_swap(__float_as_uint(x), __float_as_uint(x), false, false);
  x = fmaxf(__uint_as_float(s[0]), __uint_as_float(s[1]));
  auto t = __builtin_amdgcn_permlane32_swap(__float_as_uint(x), __float_as_uint(x), false, false);
  return fmaxf(__uint_as_float(t[0]), __uint_as_float(t[1]));
}
__device__ __forceinline__ float wave_sum(float x) {
  x += dppf<XOR1>(x); x += dppf<XOR2>(x); x += dppf<XOR7>(x); x += dppf<ROR8>(x);
  return xrow_sum(x);
}
__device__ __forceinline__ f32x4 mfma(u32x4 a, u32x4 b, f32x4 c) {
  return __builtin_amdgcn_mfma_f32_16x16x32_bf16(__builtin_bit_cast(bf16x8, a), __builtin_bit_cast(bf16x8, b), c, 0, 0, 0);
}
__device__ __forceinline__ void wg_barrier() {
  asm volatile("s_waitcnt lgkmcnt(0)" ::: "memory");
  __builtin_amdgcn_s_barrier();
  asm volatile("" ::: "memory");
}
__device__ __forceinline__ float sigmoidf_(float x) { return 1.f / (1.f + __expf(-x)); }
__device__ __forceinline__ __amdgpu_buffer_rsrc_t rsrc(const void* p, int bytes) {
  return __builtin_amdgcn_make_buffer_rsrc(const_cast<void*>(p), 0, bytes, RSRC3);
}
__device__ __forceinline__ f32x4 vm_step(u32x4 w, unsigned pp, const u32x4& msk, f32x4 d) {
  u32x4 bq = {pp & msk[0], pp & msk[1], pp & msk[2], pp & msk[3]};
  return mfma(w, bq, d);
}
__device__ __forceinline__ unsigned rep16(unsigned pair, int odd) {
  return odd ? __builtin_amdgcn_perm(pair, pair, 0x03020302u) : __builtin_amdgcn_perm(pair, pair, 0x01000100u);
}

struct Tile { u32x4 k[4], v[4]; };

__global__ void __launch_bounds__(NT, 2) fused_decode_kernel(Params p) {
  __shared__ __attribute__((aligned(16))) float lds[L_END];
  const int tid = threadIdx.x, lane = tid & 63, wave = __builtin_amdgcn_readfirstlane(tid >> 6);
  const int g = lane >> 4, c = lane & 15;
  const int bid = blockIdx.x, h = (bid & 7) * 4 + ((bid >> 3) & 3), b = bid >> 5;

  if (wave < NSW) {
    const size_t bh = (size_t)(b * NH + h) * SK * DH;
    const __amdgpu_buffer_rsrc_t rk = rsrc(p.K + bh, SK * DH * 2), rv = rsrc(p.V + bh, SK * DH * 2);
    const int voff = lane * 16;
    auto ldk = [&](int i, int j) { return __builtin_amdgcn_raw_buffer_load_b128(rk, voff, (i * NSW + wave) * 4096 + j * 1024, AUX_NT); };
    auto ldv = [&](int i, int j) { return __builtin_amdgcn_raw_buffer_load_b128(rv, voff, (i * NSW + wave) * 4096 + j * 1024, AUX_NT); };
    Tile A, E[BURST];
#pragma unroll
    for (int j = 0; j < 4; ++j) A.k[j] = ldk(0, j);
#pragma unroll
    for (int j = 0; j < 4; ++j) A.v[j] = ldv(0, j);
#pragma unroll
    for (int e = 0; e < BURST; ++e) {
#pragma unroll
      for (int j = 0; j < 4; ++j) E[e].k[j] = ldk(1 + e, j);
#pragma unroll
      for (int j = 0; j < 4; ++j) E[e].v[j] = ldv(1 + e, j);
    }
    wg_barrier();
    unsigned qp[4];
    {
      f32x4 q0 = {0, 0, 0, 0}, q1 = {0, 0, 0, 0};
#pragma unroll
      for (int w = 0; w < 4; ++w) {
        q0 += *reinterpret_cast<const f32x4*>(&lds[L_QP + w * 128 + 8 * c]);
        q1 += *reinterpret_cast<const f32x4*>(&lds[L_QP + w * 128 + 8 * c + 4]);
      }
      qp[0] = pk_bf16(q0[0], q0[1]); qp[1] = pk_bf16(q0[2], q0[3]); qp[2] = pk_bf16(q1[0], q1[1]); qp[3] = pk_bf16(q1[2], q1[3]);
    }
    float m = -INFINITY, l = 0.f, acc[8];
#pragma unroll
    for (int k = 0; k < 8; ++k) acc[k] = 0.f;
    const bool b8 = c & 8, b4 = c & 4;

    auto process = [&](Tile& T, bool refill, int inext) {
      float x[4];
#pragma unroll
      for (int j = 0; j < 4; ++j) {
        float d = dot2(T.k[j][0], qp[0], 0.f);
        d = dot2(T.k[j][1], qp[1], d); d = dot2(T.k[j][2], qp[2], d); d = dot2(T.k[j][3], qp[3], d);
        x[j] = d;
        if (refill) T.k[j] = ldk(inext, j);
      }
      const float a0 = x[0] + dppf<ROR8>(x[0]), a1 = x[1] + dppf<ROR8>(x[1]), a2 = x[2] + dppf<ROR8>(x[2]), a3 = x[3] + dppf<ROR8>(x[3]);
      const float y0 = b8 ? a2 : a0, y1 = b8 ? a3 : a1;
      const float z0 = y0 + dppf<XOR7>(y0), z1 = y1 + dppf<XOR7>(y1);
      float z = b4 ? z1 : z0;
      z += dppf<XOR2>(z); z += dppf<XOR1>(z);
      const float sc = z * C2;
      float pm = fmaxf(sc, dppf<ROR4>(sc)); pm = fmaxf(pm, dppf<ROR8>(pm));
      if (__any(pm > m)) {
        const float mn = fmaxf(m, pm), al = __builtin_amdgcn_exp2f(m - mn);
        l *= al;
#pragma unroll
        for (int k = 0; k < 8; ++k) acc[k] *= al;
        m = mn;
      }
      const float pe = __builtin_amdgcn_exp2f(sc - m);
      l += pe;
      const unsigned plo = pk_bf16(pe, 0.f), phi = plo << 16;
#define PV(j) { const unsigned lo_ = dppu<BCAST + 4 * (j)>(plo), hi_ = dppu<BCAST + 4 * (j)>(phi); \
        _Pragma("unroll") for (int k = 0; k < 4; ++k) { acc[2 * k] = dot2(T.v[j][k], lo_, acc[2 * k]); acc[2 * k + 1] = dot2(T.v[j][k], hi_, acc[2 * k + 1]); } \
        if (refill) T.v[j] = ldv(inext, j); }
      PV(0) PV(1) PV(2) PV(3)
#undef PV
    };
    process(A, true, 1 + BURST);
#pragma unroll
    for (int e = 0; e < BURST; ++e) process(E[e], false, 0);
    for (int i = 1 + BURST; i < TILES - 1; ++i) process(A, true, i + 1);
    process(A, false, 0);
    {
      const float M = xrow_max(m), sc = __builtin_amdgcn_exp2f(m - M);
      float ls = l * sc;
      ls += dppf<ROR4>(ls); ls += dppf<ROR8>(ls);
      ls = xrow_sum(ls);
      float o[8];
#pragma unroll
      for (int k = 0; k < 8; ++k) o[k] = xrow_sum(acc[k] * sc);
      float* rec = &lds[L_MRG + wave * 132];
      if (g == 0) {
        *reinterpret_cast<f32x4*>(rec + 8 * c) = f32x4{o[0], o[1], o[2], o[3]};
        *reinterpret_cast<f32x4*>(rec + 8 * c + 4) = f32x4{o[4], o[5], o[6], o[7]};
        if (c == 0) { rec[128] = M; rec[129] = ls; }
      }
    }
    wg_barrier();
    if (wave != 0) return;
    const unsigned tag = __float_as_uint(lds[L_SC + 3]);
    const __amdgpu_buffer_rsrc_t rin = rsrc(p.ws + WS_INBOX + (size_t)(b * 32 + h) * 4096, 4096);
    u32x4 gv[4];
    for (unsigned spins = 0;; ++spins) {
      bool ok = true;
#pragma unroll
      for (int q = 0; q < 4; ++q) gv[q] = __builtin_amdgcn_raw_buffer_load_b128(rin, lane * 16 + q * 1024, 0, AUX_SC1);
#pragma unroll
      for (int q = 0; q < 4; ++q) ok = ok && gv[q][1] == tag && gv[q][3] == tag;
      if (__all(ok) || spins > SPIN_CAP) break;
      __builtin_amdgcn_s_sleep(2);
    }
    float s0 = 0.f, s1 = 0.f;
#pragma unroll
    for (int q = 0; q < 4; ++q) { s0 += __uint_as_float(gv[q][0]); s1 += __uint_as_float(gv[q][2]); }
    s0 += dppf<ROR8>(s0); s1 += dppf<ROR8>(s1);
    s0 = xrow_sum(s0); s1 = xrow_sum(s1);
    if (lane < 16) {
      const int col = 16 * h + lane;
      const float s = lane < 8 ? s0 : s1;
      const float xb = __builtin_bit_cast(float, (unsigned)p.xA[b * DM + col] << 16);
      const float r = s + lds[L_SC + 1] * (xb + xb * 0.f);
      p.out[b * DM + col] = __builtin_bit_cast(unsigned short, (__bf16)r);
    }
    if (h == 0 && lane == 0) __hip_atomic_store((gu32*)(p.ws + WS_EPOCH + b * 256), tag, __ATOMIC_RELAXED, __HIP_MEMORY_SCOPE_AGENT);
    return;
  }

  const int hw = wave - NSW;
  u32x4 msk;
#pragma unroll
  for (int w = 0; w < 4; ++w) msk[w] = (c < 8 && (c >> 1) == w) ? ((c & 1) ? 0xffff0000u : 0x0000ffffu) : 0u;
  u32x4 W[32];
  {
    const __amdgpu_buffer_rsrc_t rq = rsrc(p.wq, DM * NH * DH * 2);
    const int voff = (128 * hw + 32 * g) * 8192 + h * 256 + 16 * c;
#pragma unroll
    for (int i = 0; i < 32; ++i) W[i] = __builtin_amdgcn_raw_buffer_load_b128(rq, voff, i * 8192, 0);
    u32x4 xp[4];
    const u32x4* xs = reinterpret_cast<const u32x4*>(p.xA + b * DM + 128 * hw + 32 * g);
#pragma unroll
    for (int i = 0; i < 4; ++i) xp[i] = xs[i];
    if (hw == 0 && lane == 0) {
      const unsigned e = __hip_atomic_load((gu32*)(p.ws + WS_EPOCH + b * 256), __ATOMIC_RELAXED, __HIP_MEMORY_SCOPE_AGENT);
      lds[L_SC + 3] = __uint_as_float(e + 1u);
    }
    f32x4 d = {0, 0, 0, 0};
#pragma unroll
    for (int i = 0; i < 32; ++i) d = vm_step(W[i], rep16(xp[i >> 3][(i >> 1) & 3], i & 1), msk, d);
    if (c < 8) {
#pragma unroll
      for (int r = 0; r < 4; ++r) lds[L_QP + hw * 128 + 32 * g + 8 * r + c] = d[r];
    }
  }
  wg_barrier();
  if (hw == 1) {
    const u32x4 xa = *reinterpret_cast<const u32x4*>(p.xA + b * DM + 8 * lane), gw = *reinterpret_cast<const u32x4*>(p.gw + 8 * lane);
    const u32x4 xb = *reinterpret_cast<const u32x4*>(p.xA + b * DM + 8 * lane), wr = *reinterpret_cast<const u32x4*>(p.wr + 8 * lane);
    float sg = 0.f, sr = 0.f;
#pragma unroll
    for (int k = 0; k < 4; ++k) {
      sg += rnd_bf16(bf_lo(xa[k]) * bf_lo(gw[k])) + rnd_bf16(bf_hi(xa[k]) * bf_hi(gw[k]));
      sr = dot2(xb[k], wr[k], sr);
    }
    sg = wave_sum(sg); sr = wave_sum(sr);
    const float gate = rnd_bf16(sigmoidf_(rnd_bf16(sg)));
    const float rs = rnd_bf16(sigmoidf_(rnd_bf16(sr)));
    if (lane == 0) { lds[L_SC + 0] = gate; lds[L_SC + 1] = rnd_bf16(1.f - gate); lds[L_SC + 2] = rs; }
  }
  if (hw == 0) {
    if (h < FF / 64) {
      f32x4 d = {0, 0, 0, 0};
      const unsigned short* wbase = (c < 8 ? p.wg : p.wu) + 64 * h + 8 * (c & 7) + (size_t)(32 * g) * FF;
      for (int rd = 0; rd < 4; ++rd) {
#pragma unroll
        for (int i = 0; i < 32; ++i) W[i] = *reinterpret_cast<const u32x4*>(wbase + (size_t)(128 * rd + i) * FF);
        u32x4 xp[4];
        const u32x4* xs = reinterpret_cast<const u32x4*>(p.xA + b * DM + 128 * rd + 32 * g);
#pragma unroll
        for (int i = 0; i < 4; ++i) xp[i] = xs[i];
#pragma unroll
        for (int i = 0; i < 32; ++i) d = vm_step(W[i], rep16(xp[i >> 3][(i >> 1) & 3], i & 1), msk, d);
      }
#pragma unroll
      for (int r = 0; r < 4; ++r) {
        auto sw = __builtin_amdgcn_permlane32_swap(__float_as_uint(d[r]), __float_as_uint(d[r]), false, false);
        const float gt = rnd_bf16(__uint_as_float(sw[0])), up = rnd_bf16(__uint_as_float(sw[1]));
        const float si = rnd_bf16(gt * sigmoidf_(gt));
        const unsigned hh = pk_bf16(si * up, si * up);
        if (g < 2 && c < 8) lds[L_H + 32 * g + 8 * r + c] = __uint_as_float(hh);
      }
      asm volatile("s_waitcnt lgkmcnt(0)" ::: "memory");
      u32x4 hp[4];
#pragma unroll
      for (int i = 0; i < 4; ++i) hp[i] = *reinterpret_cast<const u32x4*>(&lds[L_H + 16 * g + 4 * i]);
      const __amdgpu_buffer_rsrc_t rd_ = rsrc(p.wd, FF * DM * 2);
      const int voff = (64 * h + 16 * g) * 1024 + 16 * c;
#pragma unroll
      for (int half = 0; half < 2; ++half) {
#pragma unroll
        for (int i = 0; i < 32; ++i) W[i] = __builtin_amdgcn_raw_buffer_load_b128(rd_, voff, (i & 15) * 1024 + (2 * half + (i >> 4)) * 256, 0);
#pragma unroll
        for (int cb = 0; cb < 2; ++cb) {
          f32x4 dd = {0, 0, 0, 0};
#pragma unroll
          for (int i = 0; i < 16; ++i) dd = vm_step(W[16 * cb + i], hp[i >> 2][i & 3], msk, dd);
          if (c < 8) {
#pragma unroll
            for (int r = 0; r < 4; ++r) lds[L_Y + 128 * (2 * half + cb) + 32 * g + 8 * r + c] = dd[r];
          }
        }
      }
    } else {
#pragma unroll
      for (int i = 0; i < 8; ++i) lds[L_Y + 64 * i + lane] = 0.f;
    }
  }
  {
    const __amdgpu_buffer_rsrc_t ro = rsrc(p.wo, NH * DH * DM * 2);
    const int voff = (128 * h + 32 * g) * 1024 + 256 * hw + 16 * c;
#pragma unroll
    for (int i = 0; i < 32; ++i) W[i] = __builtin_amdgcn_raw_buffer_load_b128(ro, voff, i * 1024, 0);
  }
  wg_barrier();
  {
    float mw[4], lw[4];
#pragma unroll
    for (int w = 0; w < 4; ++w) { mw[w] = lds[L_MRG + w * 132 + 128]; lw[w] = lds[L_MRG + w * 132 + 129]; }
    const float M = fmaxf(fmaxf(mw[0], mw[1]), fmaxf(mw[2], mw[3]));
    float L = 0.f, o0 = 0.f, o1 = 0.f;
#pragma unroll
    for (int w = 0; w < 4; ++w) {
      const float wt = __builtin_amdgcn_exp2f(mw[w] - M);
      const u32x2 ov = *reinterpret_cast<const u32x2*>(&lds[L_MRG + w * 132 + 32 * g + 2 * c]);
      L = fmaf(wt, lw[w], L); o0 = fmaf(wt, __uint_as_float(ov[0]), o0); o1 = fmaf(wt, __uint_as_float(ov[1]), o1);
    }
    const float inv = 1.f / L;
    o0 *= inv; o1 *= inv;
    const unsigned slo = pk_bf16(o0, o0), shi = pk_bf16(o1, o1);
    f32x4 d0 = {0, 0, 0, 0}, d1 = {0, 0, 0, 0};
#define WO(i) d0 = vm_step(W[2 * (i)], dppu<BCAST + (i)>(slo), msk, d0); d1 = vm_step(W[2 * (i) + 1], dppu<BCAST + (i)>(shi), msk, d1);
    WO(0) WO(1) WO(2) WO(3) WO(4) WO(5) WO(6) WO(7) WO(8) WO(9) WO(10) WO(11) WO(12) WO(13) WO(14) WO(15)
#undef WO
    const float gate = lds[L_SC + 0], yc = lds[L_SC + 1] * lds[L_SC + 2];
    const unsigned tag = __float_as_uint(lds[L_SC + 3]);
    if (c < 8) {
      const __amdgpu_buffer_rsrc_t rin = rsrc(p.ws + WS_INBOX + (size_t)b * 32 * 4096, 32 * 4096);
#pragma unroll
      for (int pi = 0; pi < 2; ++pi) {
        const int col = 128 * hw + 32 * g + 16 * pi + c;
        const float v0 = gate * (d0[2 * pi] + d1[2 * pi]) + yc * lds[L_Y + col];
        const float v1 = gate * (d0[2 * pi + 1] + d1[2 * pi + 1]) + yc * lds[L_Y + col + 8];
        const u32x4 gr = {__float_as_uint(v0), tag, __float_as_uint(v1), tag};
        __builtin_amdgcn_raw_buffer_store_b128(gr, rin, (8 * hw + 2 * g + pi) * 4096 + h * 128 + 16 * c, 0, AUX_SC1);
      }
    }
  }
}

extern "C" void kernel_launch(void* const* d_in, const int* in_sizes, int n_in, void* d_out, int out_size, void* d_ws, size_t ws_size,
                              hipStream_t stream) {
  Params p;
  p.xA = (const unsigned short*)d_in[0]; p.K = (const unsigned short*)d_in[1]; p.V = (const unsigned short*)d_in[2];
  p.wq = (const unsigned short*)d_in[3]; p.wo = (const unsigned short*)d_in[4]; p.xB = (const unsigned short*)d_in[5];
  p.wr = (const unsigned short*)d_in[6]; p.wg = (const unsigned short*)d_in[7]; p.wu = (const unsigned short*)d_in[8];
  p.wd = (const unsigned short*)d_in[9]; p.gw = (const unsigned short*)d_in[10];
  p.out = (unsigned short*)d_out; p.ws = (unsigned char*)d_ws;
  fused_decode_kernel<<<NB * NH, NT, 0, stream>>>(p);
}
```

```cpp
#include <hip/hip_runtime.h>
#include <hip/hip_bf16.h>

typedef unsigned u32x4 __attribute__((ext_vector_type(4)));
typedef unsigned u32x2 __attribute__((ext_vector_type(2)));
typedef float f32x4 __attribute__((ext_vector_type(4)));
typedef __bf16 bf16x2 __attribute__((ext_vector_type(2)));
typedef __bf16 bf16x8 __attribute__((ext_vector_type(8)));
#define GAS __attribute__((address_space(1)))
#define LAS __attribute__((address_space(3)))
typedef GAS unsigned gu32;

constexpr int NB = 8, NH = 32, SK = 4096, DH = 128, DM = 512, FF = 1792;
#define NSW_ 4
#define KD 1
#define VD 1
#define BURST 3
#define HD 8
#define HPRIO 3
#define HPRIO2 1
#define HD2 4
#define POLL 1
#define POLL_FAST 32
#define POLL_SLEEP 16
#define OUT_SC1 0
#define POLL2 0
#define TIGHT 1
#define KFIRST 0
#define K_AUX 2
#define V_AUX 2
#define W_AUX 2
constexpr int NSW = NSW_;
constexpr int NT = (NSW + 4) * 64;
constexpr int TILES = SK / 16 / NSW;
constexpr int UNR = (KD == VD) ? KD : KD * VD;
constexpr float C2 = 0.08838834764831845f * 1.4426950408889634f;
constexpr int RSRC3 = 0x00020000;
constexpr int AUX_NT = 2, AUX_SC1 = 16;
constexpr int WS_EPOCH = 0, WS_INBOX = 4096;
constexpr unsigned SPIN_CAP = 1u << 16;

struct Params {
  const unsigned short *xA, *K, *V, *wq, *wo, *xB, *wr, *wg, *wu, *wd, *gw;
  unsigned short* out;
  unsigned char* ws;
};

constexpr int L_QP = 0;
constexpr int L_MRG = L_QP + 512;
constexpr int L_GU = L_MRG + NSW * 132;
constexpr int L_H = L_GU + 512;
constexpr int L_SC = L_H + 256;
constexpr int L_ST = L_SC + 8;
constexpr int L_END = L_ST + 4 * 2048;

__device__ __forceinline__ float dot2(unsigned a, unsigned b, float c) {
  return __builtin_amdgcn_fdot2_f32_bf16(__builtin_bit_cast(bf16x2, a), __builtin_bit_cast(bf16x2, b), c, false);
}
__device__ __forceinline__ unsigned pk_bf16(float lo, float hi) {
  bf16x2 v = {(__bf16)lo, (__bf16)hi};
  return __builtin_bit_cast(unsigned, v);
}
__device__ __forceinline__ float rnd_bf16(float x) { return (float)(__bf16)x; }
__device__ __forceinline__ float bf_lo(unsigned u) { return __builtin_bit_cast(float, u << 16); }
__device__ __forceinline__ float bf_hi(unsigned u) { return __builtin_bit_cast(float, u & 0xffff0000u); }
template <int CTRL> __device__ __forceinline__ float dppf(float x) {
  return __builtin_bit_cast(float, __builtin_amdgcn_mov_dpp(__builtin_bit_cast(int, x), CTRL, 0xf, 0xf, true));
}
template <int CTRL> __device__ __forceinline__ unsigned dppu(unsigned x) {
  return (unsigned)__builtin_amdgcn_mov_dpp((int)x, CTRL, 0xf, 0xf, true);
}
constexpr int XOR1 = 0xB1, XOR2 = 0x4E, XOR7 = 0x141, ROR8 = 0x128, ROR4 = 0x124, BCAST = 0x150;
__device__ __forceinline__ float xrow_sum(float x) {
  auto s = __builtin_amdgcn_permlane16_swap(__float_as_uint(x), __float_as_uint(x), false, false);
  x = __uint_as_float(s[0]) + __uint_as_float(s[1]);
  auto t = __builtin_amdgcn_permlane32_swap(__float_as_uint(x), __float_as_uint(x), false, false);
  return __uint_as_float(t[0]) + __uint_as_float(t[1]);
}
__device__ __forceinline__ float xrow_max(float x) {
  auto s = __builtin_amdgcn_permlane16_swap(__float_as_uint(x), __float_as_uint(x), false, false);
  x = fmaxf(__uint_as_float(s[0]), __uint_as_float(s[1]));
  auto t = __builtin_amdgcn_permlane32_swap(__float_as_uint(x), __float_as_uint(x), false, false);
  return fmaxf(__uint_as_float(t[0]), __uint_as_float(t[1]));
}
__device__ __forceinline__ float wave_sum(float x) {
  x += dppf<XOR1>(x); x += dppf<XOR2>(x); x += dppf<XOR7>(x); x += dppf<ROR8>(x);
  return xrow_sum(x);
}
__device__ __forceinline__ f32x4 mfma(u32x4 a, u32x4 b, f32x4 c) {
  return __builtin_amdgcn_mfma_f32_16x16x32_bf16(__builtin_bit_cast(bf16x8, a), __builtin_bit_cast(bf16x8, b), c, 0, 0, 0);
}
__device__ __forceinline__ void wg_barrier() {
  asm volatile("s_waitcnt lgkmcnt(0)" ::: "memory");
  __builtin_amdgcn_s_barrier();
  asm volatile("" ::: "memory");
}
__device__ __forceinline__ float sigmoidf_(float x) { return 1.f / (1.f + __expf(-x)); }
__device__ __forceinline__ __amdgpu_buffer_rsrc_t rsrc(const void* p, int bytes) {
  return __builtin_amdgcn_make_buffer_rsrc(const_cast<void*>(p), 0, bytes, RSRC3);
}
__device__ __forceinline__ f32x4 vm_step(u32x4 w, unsigned pp, const u32x4& msk, f32x4 d) {
  u32x4 bq = {pp & msk[0], pp & msk[1], pp & msk[2], pp & msk[3]};
  return mfma(w, bq, d);
}
__device__ __forceinline__ unsigned rep16(unsigned pair, int odd) {
  return odd ? __builtin_amdgcn_perm(pair, pair, 0x03020302u) : __builtin_amdgcn_perm(pair, pair, 0x01000100u);
}

#define STAMP_FLUSH() do {} while (0)
#define STAMP(i) do {} while (0)
__device__ __forceinline__ u32x4 ldw(const unsigned short* p) {
  return __builtin_nontemporal_load(reinterpret_cast<const u32x4*>(p));
}

__global__ void __launch_bounds__(NT, NT / 256) fused_decode_kernel(Params p) {
  __shared__ __attribute__((aligned(16))) float lds[L_END];
  const int tid = threadIdx.x, lane = tid & 63, wave = __builtin_amdgcn_readfirstlane(tid >> 6);
  const int g = lane >> 4, c = lane & 15;
  const int bid = blockIdx.x, h = (bid & 7) * 4 + ((bid >> 3) & 3), b = bid >> 5;

  STAMP(0);
#define DIAGAT(k) do {} while (0)
  if (wave < NSW) {
    const size_t bh = (size_t)(b * NH + h) * SK * DH;
    const __amdgpu_buffer_rsrc_t rk = rsrc(p.K + bh, SK * DH * 2), rv = rsrc(p.V + bh, SK * DH * 2);
    const int voff = lane * 16;
    auto ldk = [&](int i, int j) { return __builtin_amdgcn_raw_buffer_load_b128(rk, voff, (i * NSW + wave) * 4096 + j * 1024, K_AUX); };
    auto ldv = [&](int i, int j) { return __builtin_amdgcn_raw_buffer_load_b128(rv, voff, (i * NSW + wave) * 4096 + j * 1024, V_AUX); };
    u32x4 RK[KD][4], RV[VD][4], EK[BURST + 1][4], EV[BURST + 1][4];
#pragma unroll
    for (int e = 0; e < (KD > VD ? KD : VD); ++e) {
      if (e < KD) {
#pragma unroll
        for (int j = 0; j < 4; ++j) RK[e][j] = ldk(BURST + e, j);
      }
      if (e < VD) {
#pragma unroll
        for (int j = 0; j < 4; ++j) RV[e][j] = ldv(BURST + e, j);
      }
    }
#pragma unroll
    for (int e = 0; e < BURST; ++e) {
#pragma unroll
      for (int j = 0; j < 4; ++j) EK[e][j] = ldk(e, j);
#pragma unroll
      for (int j = 0; j < 4; ++j) EV[e][j] = ldv(e, j);
    }
    STAMP(7);
    wg_barrier();
    STAMP(1);
    DIAGAT(1);
    unsigned qp[4];
    {
      f32x4 q0 = {0, 0, 0, 0}, q1 = {0, 0, 0, 0};
#pragma unroll
      for (int w = 0; w < 4; ++w) {
        q0 += *reinterpret_cast<const f32x4*>(&lds[L_QP + w * 128 + 8 * c]);
        q1 += *reinterpret_cast<const f32x4*>(&lds[L_QP + w * 128 + 8 * c + 4]);
      }
      qp[0] = pk_bf16(q0[0], q0[1]); qp[1] = pk_bf16(q0[2], q0[3]); qp[2] = pk_bf16(q1[0], q1[1]); qp[3] = pk_bf16(q1[2], q1[3]);
    }
    float m = -INFINITY, l = 0.f, acc[8];
#pragma unroll
    for (int k = 0; k < 8; ++k) acc[k] = 0.f;
    const bool b8 = c & 8, b4 = c & 4;

#define PIN() __builtin_amdgcn_sched_barrier(0)
    auto process = [&](u32x4 (&TK)[4], u32x4 (&TV)[4], bool refk, int nk, bool refv, int nv) {
      float x[4];
#pragma unroll
      for (int j = 0; j < 4; ++j) {
        float d = dot2(TK[j][0], qp[0], 0.f);
        d = dot2(TK[j][1], qp[1], d); d = dot2(TK[j][2], qp[2], d); d = dot2(TK[j][3], qp[3], d);
        x[j] = d;
        if (refk) { TK[j] = ldk(nk, j); PIN(); }
      }
      const float a0 = x[0] + dppf<ROR8>(x[0]), a1 = x[1] + dppf<ROR8>(x[1]), a2 = x[2] + dppf<ROR8>(x[2]), a3 = x[3] + dppf<ROR8>(x[3]);
      const float y0 = b8 ? a2 : a0, y1 = b8 ? a3 : a1;
      const float z0 = y0 + dppf<XOR7>(y0), z1 = y1 + dppf<XOR7>(y1);
      float z = b4 ? z1 : z0;
      z += dppf<XOR2>(z); z += dppf<XOR1>(z);
      const float sc = z * C2;
      float pm = fmaxf(sc, dppf<ROR4>(sc)); pm = fmaxf(pm, dppf<ROR8>(pm));
      if (__any(pm > m)) {
        const float mn = fmaxf(m, pm), al = __builtin_amdgcn_exp2f(m - mn);
        l *= al;
#pragma unroll
        for (int k = 0; k < 8; ++k) acc[k] *= al;
        m = mn;
      }
      const float pe = __builtin_amdgcn_exp2f(sc - m);
      l += pe;
      const unsigned plo = pk_bf16(pe, 0.f), phi = plo << 16;
#define PV(j) { const unsigned lo_ = dppu<BCAST + 4 * (j)>(plo), hi_ = dppu<BCAST + 4 * (j)>(phi); \
        _Pragma("unroll") for (int k = 0; k < 4; ++k) { acc[2 * k] = dot2(TV[j][k], lo_, acc[2 * k]); acc[2 * k + 1] = dot2(TV[j][k], hi_, acc[2 * k + 1]); } \
        if (refv) { TV[j] = ldv(nv, j); PIN(); } }
      PV(0) PV(1) PV(2) PV(3)
#undef PV
    };
    static_assert((TILES - BURST) % UNR == 0 && TILES - BURST >= 3 * UNR, "ring rounds");
#pragma unroll
    for (int e = 0; e < UNR; ++e) process(RK[e % KD], RV[e % VD], true, BURST + e + KD, true, BURST + e + VD);
    STAMP(2);
    DIAGAT(2);
#pragma unroll
    for (int e = 0; e < BURST; ++e) process(EK[e], EV[e], false, 0, false, 0);
    constexpr int DMAX = KD > VD ? KD : VD;
    constexpr int LAST = TILES - ((DMAX + UNR - 1) / UNR) * UNR;
    for (int i = BURST + UNR; i < LAST; i += UNR) {
#pragma unroll
      for (int e = 0; e < UNR; ++e) process(RK[e % KD], RV[e % VD], true, i + e + KD, true, i + e + VD);
    }
#pragma unroll
    for (int t = LAST; t < TILES; ++t) process(RK[(t - BURST) % KD], RV[(t - BURST) % VD], t + KD < TILES, t + KD, t + VD < TILES, t + VD);
    STAMP(3);
    DIAGAT(3);
    {
      const float M = xrow_max(m), sc = __builtin_amdgcn_exp2f(m - M);
      float ls = l * sc;
      ls += dppf<ROR4>(ls); ls += dppf<ROR8>(ls);
      ls = xrow_sum(ls);
      float o[8];
#pragma unroll
      for (int k = 0; k < 8; ++k) o[k] = xrow_sum(acc[k] * sc);
      float* rec = &lds[L_MRG + wave * 132];
      if (g == 0) {
        *reinterpret_cast<f32x4*>(rec + 8 * c) = f32x4{o[0], o[1], o[2], o[3]};
        *reinterpret_cast<f32x4*>(rec + 8 * c + 4) = f32x4{o[4], o[5], o[6], o[7]};
        if (c == 0) { rec[128] = M; rec[129] = ls; }
      }
    }
    unsigned xfin = 0;
    if (wave == 0 && lane < 16) xfin = p.xA[b * DM + 16 * h + lane];
    wg_barrier();
    STAMP(4);
    if (wave != 0) { STAMP_FLUSH(); return; }
    const unsigned tag = __float_as_uint(lds[L_SC + 3]);
    const __amdgpu_buffer_rsrc_t rin = rsrc(p.ws + WS_INBOX + (size_t)(b * 32 + h) * 4096, 4096);
    u32x4 gv[4];
#pragma unroll
    for (int q = 0; q < 4; ++q) gv[q] = u32x4{0u, ~tag, 0u, ~tag};
    for (unsigned spins = 0;; ++spins) {
#pragma unroll
      for (int q = 0; q < 4; ++q)
        if (gv[q][1] != tag || gv[q][3] != tag) gv[q] = __builtin_amdgcn_raw_buffer_load_b128(rin, lane * 16 + q * 1024, 0, AUX_SC1);
      int missing = 0;
#pragma unroll
      for (int q = 0; q < 4; ++q) missing += __popcll(__ballot(gv[q][1] != tag || gv[q][3] != tag));
      if (missing == 0 || spins > SPIN_CAP) break;
      if (missing > POLL_FAST) __builtin_amdgcn_s_sleep(POLL_SLEEP);
    }
    STAMP(5);
    float s0 = 0.f, s1 = 0.f;
#pragma unroll
    for (int q = 0; q < 4; ++q) { s0 += __uint_as_float(gv[q][0]); s1 += __uint_as_float(gv[q][2]); }
    s0 += dppf<ROR8>(s0); s1 += dppf<ROR8>(s1);
    s0 = xrow_sum(s0); s1 = xrow_sum(s1);
    if (lane < 16) {
      const int col = 16 * h + lane;
      const float s = lane < 8 ? s0 : s1;
      const float xb = __builtin_bit_cast(float, xfin << 16);
      const float r = s + lds[L_SC + 1] * (xb + xb * 0.f);
      p.out[b * DM + col] = __builtin_bit_cast(unsigned short, (__bf16)r);
    }
    STAMP(6);
    if (h == 0 && lane == 0) __hip_atomic_store((gu32*)(p.ws + WS_EPOCH + b * 256), tag, __ATOMIC_RELAXED, __HIP_MEMORY_SCOPE_AGENT);
    STAMP_FLUSH();
    return;
  }

  const int hw = wave - NSW;
  __builtin_amdgcn_s_setprio(HPRIO);
  u32x4 msk;
#pragma unroll
  for (int w = 0; w < 4; ++w) msk[w] = (c < 8 && (c >> 1) == w) ? ((c & 1) ? 0xffff0000u : 0x0000ffffu) : 0u;
  u32x4 W[16], xp[4];
  {
    const __amdgpu_buffer_rsrc_t rq = rsrc(p.wq, DM * NH * DH * 2);
    const u32x4* xs = reinterpret_cast<const u32x4*>(p.xA + b * DM + 128 * hw + 32 * g);
#pragma unroll
    for (int i = 0; i < 4; ++i) xp[i] = xs[i];
    const int voff = (128 * hw + 32 * g) * 8192 + h * 256 + 16 * c;
#pragma unroll
#define WQLD(o) __builtin_amdgcn_raw_buffer_load_b128(rq, voff, (o), W_AUX)
    for (int i = 0; i < HD; ++i) W[i] = WQLD(i * 8192);
    STAMP(8);
    if (hw == 0 && lane == 0) lds[L_SC + 4] = 0.f;
    f32x4 d = {0, 0, 0, 0};
#pragma unroll
    for (int i = 0; i < 32; ++i) {
      d = vm_step(W[i % HD], rep16(xp[i >> 3][(i >> 1) & 3], i & 1), msk, d);
      if (i + HD < 32) W[i % HD] = WQLD((i + HD) * 8192);
      __builtin_amdgcn_sched_barrier(0);
    }
    if (c < 8) {
#pragma unroll
      for (int r = 0; r < 4; ++r) lds[L_QP + hw * 128 + 32 * g + 8 * r + c] = d[r];
    }
  }
  STAMP(1);
  __builtin_amdgcn_s_setprio(HPRIO2);
  wg_barrier();
  if (hw == 2 && lane == 0) {
    const unsigned epoch = __hip_atomic_load((gu32*)(p.ws + WS_EPOCH + b * 256), __ATOMIC_RELAXED, __HIP_MEMORY_SCOPE_AGENT);
    lds[L_SC + 3] = __uint_as_float(epoch + 1u);
  }
  if (hw == 3) {
    const u32x4 xa = *reinterpret_cast<const u32x4*>(p.xA + b * DM + 8 * lane), gw = *reinterpret_cast<const u32x4*>(p.gw + 8 * lane);
    const u32x4 wr = *reinterpret_cast<const u32x4*>(p.wr + 8 * lane);
    float sg = 0.f, sr = 0.f;
#pragma unroll
    for (int k = 0; k < 4; ++k) {
      sg += rnd_bf16(bf_lo(xa[k]) * bf_lo(gw[k])) + rnd_bf16(bf_hi(xa[k]) * bf_hi(gw[k]));
      sr = dot2(xa[k], wr[k], sr);
    }
    sg = wave_sum(sg); sr = wave_sum(sr);
    const float gate = rnd_bf16(sigmoidf_(rnd_bf16(sg)));
    const float rs = rnd_bf16(sigmoidf_(rnd_bf16(sr)));
    if (lane == 0) { lds[L_SC + 0] = gate; lds[L_SC + 1] = rnd_bf16(1.f - gate); lds[L_SC + 2] = rs; }
  }
  f32x4 dy = {0, 0, 0, 0};
  if (h < FF / 64) {
    {
      f32x4 d = {0, 0, 0, 0};
      const unsigned short* wbase = (c < 8 ? p.wg : p.wu) + 64 * h + 8 * (c & 7) + (size_t)(128 * hw + 32 * g) * FF;
#pragma unroll
      for (int i = 0; i < HD2; ++i) W[i] = ldw(wbase + (size_t)i * FF);
#pragma unroll
      for (int i = 0; i < 32; ++i) {
        d = vm_step(W[i % HD2], rep16(xp[i >> 3][(i >> 1) & 3], i & 1), msk, d);
        if (i + HD2 < 32) W[i % HD2] = ldw(wbase + (size_t)(i + HD2) * FF);
        __builtin_amdgcn_sched_barrier(0);
      }
      if (c < 8) {
#pragma unroll
        for (int r = 0; r < 4; ++r) lds[L_GU + hw * 128 + 32 * g + 8 * r + c] = d[r];
      }
      asm volatile("s_waitcnt lgkmcnt(0)" ::: "memory");
      if (lane == 0) __hip_atomic_fetch_add((LAS unsigned*)&lds[L_SC + 4], 1u, __ATOMIC_RELAXED, __HIP_MEMORY_SCOPE_WORKGROUP);
    }
    const __amdgpu_buffer_rsrc_t rd_ = rsrc(p.wd, FF * DM * 2);
    const int voff = (64 * h + 16 * g) * 1024 + 256 * hw + 16 * c;
#pragma unroll
    for (int i = 0; i < 16; ++i) {
      W[i] = __builtin_amdgcn_raw_buffer_load_b128(rd_, voff, i * 1024, W_AUX);
      if ((i % HD2) == HD2 - 1) asm volatile("s_waitcnt vmcnt(0)" ::: "memory");
    }
    while (__hip_atomic_load((LAS unsigned*)&lds[L_SC + 4], __ATOMIC_RELAXED, __HIP_MEMORY_SCOPE_WORKGROUP) < 4u) __builtin_amdgcn_s_sleep(4);
    asm volatile("" ::: "memory");
    {
      float gt = 0.f, up = 0.f;
#pragma unroll
      for (int w = 0; w < 4; ++w) { gt += lds[L_GU + w * 128 + lane]; up += lds[L_GU + w * 128 + 64 + lane]; }
      gt = rnd_bf16(gt); up = rnd_bf16(up);
      const float si = rnd_bf16(gt * sigmoidf_(gt));
      lds[L_H + hw * 64 + lane] = __uint_as_float(pk_bf16(si * up, si * up));
      asm volatile("s_waitcnt lgkmcnt(0)" ::: "memory");
      u32x4 hp[4];
#pragma unroll
      for (int i = 0; i < 4; ++i) hp[i] = *reinterpret_cast<const u32x4*>(&lds[L_H + hw * 64 + 16 * g + 4 * i]);
#pragma unroll
      for (int i = 0; i < 16; ++i) dy = vm_step(W[i], hp[i >> 2][i & 3], msk, dy);
    }
  }
  STAMP(2);
  u32x4 WB[8][4];
  {
    const __amdgpu_buffer_rsrc_t ro = rsrc(p.wo, NH * DH * DM * 2);
    const int voff = (128 * h + g) * 1024 + 256 * hw + 16 * c;
    float* st = &lds[L_ST + hw * 2048];
#pragma unroll
    for (int s = 0; s < 4; ++s) {
#pragma unroll
      for (int hf = 0; hf < 2; ++hf) {
#pragma unroll
        for (int i = 0; i < 4; ++i) W[i] = __builtin_amdgcn_raw_buffer_load_b128(ro, voff, (32 * s + 16 * hf + 4 * i) * 1024, W_AUX);
#pragma unroll
        for (int i = 0; i < 4; ++i) *reinterpret_cast<u32x4*>(&st[(16 * hf + 4 * i + g) * 64 + 4 * c]) = W[i];
      }
      asm volatile("s_waitcnt lgkmcnt(0)" ::: "memory");
#pragma unroll
      for (int t = 0; t < 8; ++t) {
        typedef short s16x4 __attribute__((ext_vector_type(4)));
        const float* a0 = &st[(8 * g + (c >> 2)) * 64 + 8 * t + 2 * (c & 3)];
        const s16x4 u0 = __builtin_amdgcn_ds_read_tr16_b64_v4i16((LAS s16x4*)a0);
        const s16x4 u1 = __builtin_amdgcn_ds_read_tr16_b64_v4i16((LAS s16x4*)(a0 + 4 * 64));
        const u32x2 x0 = __builtin_bit_cast(u32x2, u0), x1 = __builtin_bit_cast(u32x2, u1);
        WB[t][s] = u32x4{x0[0], x0[1], x1[0], x1[1]};
      }
      asm volatile("s_waitcnt lgkmcnt(0)" ::: "memory");
    }
  }
  STAMP(3);
  wg_barrier();
  STAMP(4);
  {
    float mw[NSW], lw[NSW];
#pragma unroll
    for (int w = 0; w < NSW; ++w) { mw[w] = lds[L_MRG + w * 132 + 128]; lw[w] = lds[L_MRG + w * 132 + 129]; }
    float M = mw[0];
#pragma unroll
    for (int w = 1; w < NSW; ++w) M = fmaxf(M, mw[w]);
    float L = 0.f, o0 = 0.f, o1 = 0.f;
#pragma unroll
    for (int w = 0; w < NSW; ++w) {
      const float wt = __builtin_amdgcn_exp2f(mw[w] - M);
      const u32x2 ov = *reinterpret_cast<const u32x2*>(&lds[L_MRG + w * 132 + 32 * g + 2 * c]);
      L = fmaf(wt, lw[w], L); o0 = fmaf(wt, __uint_as_float(ov[0]), o0); o1 = fmaf(wt, __uint_as_float(ov[1]), o1);
    }
    const float inv = __builtin_amdgcn_rcpf(L);
    float* ob = &lds[L_H + hw * 64];
    ob[16 * g + c] = __uint_as_float(pk_bf16(o0 * inv, o1 * inv));
    asm volatile("s_waitcnt lgkmcnt(0)" ::: "memory");
    u32x4 oa[4];
#pragma unroll
    for (int s = 0; s < 4; ++s) oa[s] = *reinterpret_cast<const u32x4*>(&ob[16 * s + 4 * g]);
    f32x4 d[8];
#pragma unroll
    for (int t = 0; t < 8; ++t) d[t] = f32x4{0, 0, 0, 0};
#pragma unroll
    for (int s = 0; s < 4; ++s)
#pragma unroll
      for (int t = 0; t < 8; ++t) d[t] = mfma(oa[s], WB[t][s], d[t]);
    unsigned m1 = (g & 1) ? ~0u : 0u, m2 = (g & 2) ? ~0u : 0u;
    asm volatile("" : "+v"(m1), "+v"(m2));
    const float gate = lds[L_SC + 0], yc = lds[L_SC + 1] * lds[L_SC + 2];
    const unsigned tag = __float_as_uint(lds[L_SC + 3]);
    const __amdgpu_buffer_rsrc_t rin = rsrc(p.ws + WS_INBOX + (size_t)b * 32 * 4096, 32 * 4096);
#pragma unroll
    for (int pi = 0; pi < 2; ++pi) {
      const unsigned lo = (m1 & __float_as_uint(d[2 + pi][0])) | (~m1 & __float_as_uint(d[pi][0]));
      const unsigned hi = (m1 & __float_as_uint(d[6 + pi][0])) | (~m1 & __float_as_uint(d[4 + pi][0]));
      const float a0 = __uint_as_float((m2 & hi) | (~m2 & lo));
      const float a1 = dppf<ROR8>(a0);
      if (c < 8) {
        const float v0 = gate * a0 + yc * dy[2 * pi], v1 = gate * a1 + yc * dy[2 * pi + 1];
        const u32x4 gr = {__float_as_uint(v0), tag, __float_as_uint(v1), tag};
        __builtin_amdgcn_raw_buffer_store_b128(gr, rin, (8 * hw + 2 * g + pi) * 4096 + h * 128 + 16 * c, 0, AUX_SC1);
      }
    }
    STAMP(5);
    STAMP_FLUSH();
  }
}

extern "C" void kernel_launch(void* const* d_in, const int* in_sizes, int n_in, void* d_out, int out_size, void* d_ws, size_t ws_size,
                              hipStream_t stream) {
  Params p;
  p.xA = (const unsigned short*)d_in[0]; p.K = (const unsigned short*)d_in[1]; p.V = (const unsigned short*)d_in[2];
  p.wq = (const unsigned short*)d_in[3]; p.wo = (const unsigned short*)d_in[4]; p.xB = (const unsigned short*)d_in[5];
  p.wr = (const unsigned short*)d_in[6]; p.wg = (const unsigned short*)d_in[7]; p.wu = (const unsigned short*)d_in[8];
  p.wd = (const unsigned short*)d_in[9]; p.gw = (const unsigned short*)d_in[10];
  p.out = (unsigned short*)d_out; p.ws = (unsigned char*)d_ws;
  fused_decode_kernel<<<NB * NH, NT, 0, stream>>>(p);
}
```

```cpp
#include <hip/hip_runtime.h>
#include <hip/hip_bf16.h>

typedef unsigned u32x4 __attribute__((ext_vector_type(4)));
typedef unsigned u32x2 __attribute__((ext_vector_type(2)));
typedef float f32x4 __attribute__((ext_vector_type(4)));
typedef short s16x4 __attribute__((ext_vector_type(4)));
typedef __bf16 bf16x2 __attribute__((ext_vector_type(2)));
typedef __bf16 bf16x8 __attribute__((ext_vector_type(8)));
#define GAS __attribute__((address_space(1)))
#define LAS __attribute__((address_space(3)))
typedef GAS unsigned gu32;

constexpr int NB = 8, NH = 32, SK = 4096, DH = 128, DM = 512, FF = 1792;
constexpr int NSW = 4;
constexpr int NT = (NSW + 4) * 64;
constexpr int TILES = SK / 16 / NSW;
constexpr int BURST = 3;
constexpr int HD = 8;
constexpr int HD2 = 4;
constexpr float C2 = 0.08838834764831845f * 1.4426950408889634f;
constexpr int RSRC3 = 0x00020000;
constexpr int AUX_NT = 2, AUX_SC1 = 16;
constexpr int WS_EPOCH = 0, WS_INBOX = 4096;
constexpr unsigned SPIN_CAP = 1u << 16;

struct Params {
  const unsigned short *K, *V, *xA, *wq, *wo, *wr, *wg, *wu, *wd, *gw;
  unsigned short* out;
  unsigned char* ws;
};

constexpr int L_QP = 0;
constexpr int L_MRG = L_QP + 512;
constexpr int L_GU = L_MRG + NSW * 132;
constexpr int L_H = L_GU + 512;
constexpr int L_SC = L_H + 256;
constexpr int L_ST = L_SC + 8;
constexpr int L_END = L_ST + 4 * 2048;

__device__ __forceinline__ float dot2(unsigned a, unsigned b, float c) {
  return __builtin_amdgcn_fdot2_f32_bf16(__builtin_bit_cast(bf16x2, a), __builtin_bit_cast(bf16x2, b), c, false);
}
__device__ __forceinline__ unsigned pk_bf16(float lo, float hi) {
  bf16x2 v = {(__bf16)lo, (__bf16)hi};
  return __builtin_bit_cast(unsigned, v);
}
__device__ __forceinline__ float rnd_bf16(float x) { return (float)(__bf16)x; }
__device__ __forceinline__ float bf_lo(unsigned u) { return __builtin_bit_cast(float, u << 16); }
__device__ __forceinline__ float bf_hi(unsigned u) { return __builtin_bit_cast(float, u & 0xffff0000u); }
template <int CTRL> __device__ __forceinline__ float dppf(float x) {
  return __builtin_bit_cast(float, __builtin_amdgcn_mov_dpp(__builtin_bit_cast(int, x), CTRL, 0xf, 0xf, true));
}
template <int CTRL> __device__ __forceinline__ unsigned dppu(unsigned x) {
  return (unsigned)__builtin_amdgcn_mov_dpp((int)x, CTRL, 0xf, 0xf, true);
}
constexpr int XOR1 = 0xB1, XOR2 = 0x4E, XOR7 = 0x141, ROR8 = 0x128, ROR4 = 0x124, BCAST = 0x150;
__device__ __forceinline__ float xrow_sum(float x) {
  auto s = __builtin_amdgcn_permlane16_swap(__float_as_uint(x), __float_as_uint(x), false, false);
  x = __uint_as_float(s[0]) + __uint_as_float(s[1]);
  auto t = __builtin_amdgcn_permlane32_swap(__float_as_uint(x), __float_as_uint(x), false, false);
  return __uint_as_float(t[0]) + __uint_as_float(t[1]);
}
__device__ __forceinline__ float xrow_max(float x) {
  auto s = __builtin_amdgcn_permlane16_swap(__float_as_uint(x), __float_as_uint(x), false, false);
  x = fmaxf(__uint_as_float(s[0]), __uint_as_float(s[1]));
  auto t = __builtin_amdgcn_permlane32_swap(__float_as_uint(x), __float_as_uint(x), false, false);
  return fmaxf(__uint_as_float(t[0]), __uint_as_float(t[1]));
}
__device__ __forceinline__ float wave_sum(float x) {
  x += dppf<XOR1>(x); x += dppf<XOR2>(x); x += dppf<XOR7>(x); x += dppf<ROR8>(x);
  return xrow_sum(x);
}
__device__ __forceinline__ f32x4 mfma(u32x4 a, u32x4 b, f32x4 c) {
  return __builtin_amdgcn_mfma_f32_16x16x32_bf16(__builtin_bit_cast(bf16x8, a), __builtin_bit_cast(bf16x8, b), c, 0, 0, 0);
}
__device__ __forceinline__ void wg_barrier() {
  asm volatile("s_waitcnt lgkmcnt(0)" ::: "memory");
  __builtin_amdgcn_s_barrier();
  asm volatile("" ::: "memory");
}
__device__ __forceinline__ float sigmoidf_(float x) { return 1.f / (1.f + __expf(-x)); }
__device__ __forceinline__ float safe_max(float m) { return m == -INFINITY ? 0.f : m; }
__device__ __forceinline__ __amdgpu_buffer_rsrc_t rsrc(const void* p, int bytes) {
  return __builtin_amdgcn_make_buffer_rsrc(const_cast<void*>(p), 0, bytes, RSRC3);
}
__device__ __forceinline__ f32x4 vm_step(u32x4 w, unsigned pp, const u32x4& msk, f32x4 d) {
  u32x4 bq = {pp & msk[0], pp & msk[1], pp & msk[2], pp & msk[3]};
  return mfma(w, bq, d);
}
__device__ __forceinline__ unsigned rep16(unsigned pair, int odd) {
  return odd ? __builtin_amdgcn_perm(pair, pair, 0x03020302u) : __builtin_amdgcn_perm(pair, pair, 0x01000100u);
}
__device__ __forceinline__ u32x4 ldw(const unsigned short* p) { return __builtin_nontemporal_load(reinterpret_cast<const u32x4*>(p)); }
#define PIN() __builtin_amdgcn_sched_barrier(0)

#define STAMP_FLUSH() do {} while (0)
#define STAMP(i) do {} while (0)

__global__ void __launch_bounds__(NT, NT / 256) fused_decode_kernel(Params p) {
  __shared__ __attribute__((aligned(16))) float lds[L_END];
  const int tid = threadIdx.x, lane = tid & 63, wave = __builtin_amdgcn_readfirstlane(tid >> 6);
  const int g = lane >> 4, c = lane & 15;
  const int bid = blockIdx.x, h = (bid & 7) * 4 + ((bid >> 3) & 3), b = bid >> 5;
  STAMP(0);
  const unsigned short *Kp = p.K, *Vp = p.V;
  asm volatile("" : "+s"(Kp), "+s"(Vp));

  if (__builtin_expect(wave < NSW, 1)) {
    const size_t bh = (size_t)(b * NH + h) * SK * DH;
    const __amdgpu_buffer_rsrc_t rk = rsrc(Kp + bh, SK * DH * 2), rv = rsrc(Vp + bh, SK * DH * 2);
    const int voff = lane * 16;
    auto ldk = [&](int i, int j) { return __builtin_amdgcn_raw_buffer_load_b128(rk, voff, (i * NSW + wave) * 4096 + j * 1024, AUX_NT); };
    auto ldv = [&](int i, int j) { return __builtin_amdgcn_raw_buffer_load_b128(rv, voff, (i * NSW + wave) * 4096 + j * 1024, AUX_NT); };
    u32x4 RK[4], RV[4], EK[BURST][4], EV[BURST][4];
#pragma unroll
    for (int j = 0; j < 4; ++j) RK[j] = ldk(BURST, j);
#pragma unroll
    for (int j = 0; j < 4; ++j) RV[j] = ldv(BURST, j);
#pragma unroll
    for (int e = 0; e < BURST; ++e) {
#pragma unroll
      for (int j = 0; j < 4; ++j) EK[e][j] = ldk(e, j);
#pragma unroll
      for (int j = 0; j < 4; ++j) EV[e][j] = ldv(e, j);
    }
    STAMP(7);
    wg_barrier();
    STAMP(1);
    unsigned qp[4];
    {
      f32x4 q0 = {0, 0, 0, 0}, q1 = {0, 0, 0, 0};
#pragma unroll
      for (int w = 0; w < 4; ++w) {
        q0 += *reinterpret_cast<const f32x4*>(&lds[L_QP + w * 128 + 8 * c]);
        q1 += *reinterpret_cast<const f32x4*>(&lds[L_QP + w * 128 + 8 * c + 4]);
      }
      qp[0] = pk_bf16(q0[0], q0[1]); qp[1] = pk_bf16(q0[2], q0[3]); qp[2] = pk_bf16(q1[0], q1[1]); qp[3] = pk_bf16(q1[2], q1[3]);
    }
    float m = -INFINITY, ms = 0.f, l = 0.f, acc[8];
#pragma unroll
    for (int k = 0; k < 8; ++k) acc[k] = 0.f;
    const bool b8 = c & 8, b4 = c & 4;

    auto process = [&](u32x4 (&TK)[4], u32x4 (&TV)[4], bool refill, int inext) {
      float x[4];
#pragma unroll
      for (int j = 0; j < 4; ++j) {
        float d = dot2(TK[j][0], qp[0], 0.f);
        d = dot2(TK[j][1], qp[1], d); d = dot2(TK[j][2], qp[2], d); d = dot2(TK[j][3], qp[3], d);
        x[j] = d;
        if (refill) { TK[j] = ldk(inext, j); PIN(); }
      }
      const float a0 = x[0] + dppf<ROR8>(x[0]), a1 = x[1] + dppf<ROR8>(x[1]), a2 = x[2] + dppf<ROR8>(x[2]), a3 = x[3] + dppf<ROR8>(x[3]);
      const float y0 = b8 ? a2 : a0, y1 = b8 ? a3 : a1;
      const float z0 = y0 + dppf<XOR7>(y0), z1 = y1 + dppf<XOR7>(y1);
      float z = b4 ? z1 : z0;
      z += dppf<XOR2>(z); z += dppf<XOR1>(z);
      const float sc = z * C2;
      float pm = fmaxf(sc, dppf<ROR4>(sc)); pm = fmaxf(pm, dppf<ROR8>(pm));
      if (__any(pm > m)) {
        const float mn = fmaxf(m, pm), msn = safe_max(mn), al = __builtin_amdgcn_exp2f(m == -INFINITY ? -INFINITY : m - msn);
        l *= al;
#pragma unroll
        for (int k = 0; k < 8; ++k) acc[k] *= al;
        m = mn; ms = msn;
      }
      const float pe = __builtin_amdgcn_exp2f(sc - ms);
      l += pe;
      const unsigned plo = pk_bf16(pe, 0.f), phi = plo << 16;
#define PV(j) { const unsigned lo_ = dppu<BCAST + 4 * (j)>(plo), hi_ = dppu<BCAST + 4 * (j)>(phi); \
        _Pragma("unroll") for (int k = 0; k < 4; ++k) { acc[2 * k] = dot2(TV[j][k], lo_, acc[2 * k]); acc[2 * k + 1] = dot2(TV[j][k], hi_, acc[2 * k + 1]); } \
        if (refill) { TV[j] = ldv(inext, j); PIN(); } }
      PV(0) PV(1) PV(2) PV(3)
#undef PV
    };
    process(RK, RV, true, BURST + 1);
    STAMP(2);
#pragma unroll
    for (int e = 0; e < BURST; ++e) process(EK[e], EV[e], false, 0);
    for (int i = BURST + 1; i < TILES - 1; ++i) process(RK, RV, true, i + 1);
    process(RK, RV, false, 0);
    STAMP(3);
    unsigned xfin = 0;
    if (wave == 0 && lane < 16) xfin = p.xA[b * DM + 16 * h + lane];
    {
      const float M = xrow_max(m), sc = __builtin_amdgcn_exp2f(m - safe_max(M));
      float ls = l * sc;
      ls += dppf<ROR4>(ls); ls += dppf<ROR8>(ls);
      ls = xrow_sum(ls);
      float o[8];
#pragma unroll
      for (int k = 0; k < 8; ++k) o[k] = xrow_sum(acc[k] * sc);
      float* rec = &lds[L_MRG + wave * 132];
      if (g == 0) {
        *reinterpret_cast<f32x4*>(rec + 8 * c) = f32x4{o[0], o[1], o[2], o[3]};
        *reinterpret_cast<f32x4*>(rec + 8 * c + 4) = f32x4{o[4], o[5], o[6], o[7]};
        if (c == 0) { rec[128] = M; rec[129] = ls; }
      }
    }
    wg_barrier();
    STAMP(4);
    if (wave != 0) { STAMP_FLUSH(); return; }
    const unsigned tag = __float_as_uint(lds[L_SC + 3]);
    const __amdgpu_buffer_rsrc_t rin = rsrc(p.ws + WS_INBOX + (size_t)(b * 32 + h) * 4096, 4096);
    u32x4 gv[4];
#pragma unroll
    for (int q = 0; q < 4; ++q) gv[q] = u32x4{0u, ~tag, 0u, ~tag};
    for (unsigned spins = 0;; ++spins) {
#pragma unroll
      for (int q = 0; q < 4; ++q)
        if (gv[q][1] != tag || gv[q][3] != tag) gv[q] = __builtin_amdgcn_raw_buffer_load_b128(rin, lane * 16 + q * 1024, 0, AUX_SC1);
      int missing = 0;
#pragma unroll
      for (int q = 0; q < 4; ++q) missing += __popcll(__ballot(gv[q][1] != tag || gv[q][3] != tag));
      if (missing == 0 || spins > SPIN_CAP) break;
      if (missing > 32) __builtin_amdgcn_s_sleep(16);
    }
    STAMP(5);
    float s0 = 0.f, s1 = 0.f;
#pragma unroll
    for (int q = 0; q < 4; ++q) { s0 += __uint_as_float(gv[q][0]); s1 += __uint_as_float(gv[q][2]); }
    s0 += dppf<ROR8>(s0); s1 += dppf<ROR8>(s1);
    s0 = xrow_sum(s0); s1 = xrow_sum(s1);
    if (lane < 16) {
      const float s = lane < 8 ? s0 : s1;
      const float xb = __builtin_bit_cast(float, xfin << 16);
      const float r = s + lds[L_SC + 1] * (xb + xb * 0.f);
      p.out[b * DM + 16 * h + lane] = __builtin_bit_cast(unsigned short, (__bf16)r);
    }
    STAMP(6);
    if (h == 0 && lane == 0) __hip_atomic_store((gu32*)(p.ws + WS_EPOCH + b * 256), tag, __ATOMIC_RELAXED, __HIP_MEMORY_SCOPE_AGENT);
    STAMP_FLUSH();
    return;
  }

  const int hw = wave - NSW;
  __builtin_amdgcn_s_setprio(3);
  u32x4 msk;
#pragma unroll
  for (int w = 0; w < 4; ++w) msk[w] = (c < 8 && (c >> 1) == w) ? ((c & 1) ? 0xffff0000u : 0x0000ffffu) : 0u;
  u32x4 W[16], xp[4];
  {
    const __amdgpu_buffer_rsrc_t rq = rsrc(p.wq, DM * NH * DH * 2);
    const u32x4* xs = reinterpret_cast<const u32x4*>(p.xA + b * DM + 128 * hw + 32 * g);
#pragma unroll
    for (int i = 0; i < 4; ++i) xp[i] = xs[i];
    const int voff = (128 * hw + 32 * g) * 8192 + h * 256 + 16 * c;
#pragma unroll
    for (int i = 0; i < HD; ++i) W[i] = __builtin_amdgcn_raw_buffer_load_b128(rq, voff, i * 8192, AUX_NT);
    if (hw == 0 && lane == 0) lds[L_SC + 4] = 0.f;
    f32x4 d = {0, 0, 0, 0};
#pragma unroll
    for (int i = 0; i < 32; ++i) {
      d = vm_step(W[i % HD], rep16(xp[i >> 3][(i >> 1) & 3], i & 1), msk, d);
      if (i + HD < 32) W[i % HD] = __builtin_amdgcn_raw_buffer_load_b128(rq, voff, (i + HD) * 8192, AUX_NT);
      PIN();
    }
    if (c < 8) {
#pragma unroll
      for (int r = 0; r < 4; ++r) lds[L_QP + hw * 128 + 32 * g + 8 * r + c] = d[r];
    }
  }
  STAMP(1);
  __builtin_amdgcn_s_setprio(1);
  wg_barrier();
  if (hw == 2 && lane == 0) {
    const unsigned epoch = __hip_atomic_load((gu32*)(p.ws + WS_EPOCH + b * 256), __ATOMIC_RELAXED, __HIP_MEMORY_SCOPE_AGENT);
    lds[L_SC + 3] = __uint_as_float(epoch + 1u);
  }
  if (hw == 3) {
    const u32x4 xa = *reinterpret_cast<const u32x4*>(p.xA + b * DM + 8 * lane), gw = *reinterpret_cast<const u32x4*>(p.gw + 8 * lane);
    const u32x4 wr = *reinterpret_cast<const u32x4*>(p.wr + 8 * lane);
    float sg = 0.f, sr = 0.f;
#pragma unroll
    for (int k = 0; k < 4; ++k) {
      sg += rnd_bf16(bf_lo(xa[k]) * bf_lo(gw[k])) + rnd_bf16(bf_hi(xa[k]) * bf_hi(gw[k]));
      sr = dot2(xa[k], wr[k], sr);
    }
    sg = wave_sum(sg); sr = wave_sum(sr);
    const float gate = rnd_bf16(sigmoidf_(rnd_bf16(sg)));
    const float rs = rnd_bf16(sigmoidf_(rnd_bf16(sr)));
    if (lane == 0) { lds[L_SC + 0] = gate; lds[L_SC + 1] = rnd_bf16(1.f - gate); lds[L_SC + 2] = rs; }
  }
  f32x4 dy = {0, 0, 0, 0};
  if (h < FF / 64) {
    {
      f32x4 d = {0, 0, 0, 0};
      const unsigned short* wbase = (c < 8 ? p.wg : p.wu) + 64 * h + 8 * (c & 7) + (size_t)(128 * hw + 32 * g) * FF;
#pragma unroll
      for (int i = 0; i < HD2; ++i) W[i] = ldw(wbase + (size_t)i * FF);
#pragma unroll
      for (int i = 0; i < 32; ++i) {
        d = vm_step(W[i % HD2], rep16(xp[i >> 3][(i >> 1) & 3], i & 1), msk, d);
        if (i + HD2 < 32) W[i % HD2] = ldw(wbase + (size_t)(i + HD2) * FF);
        PIN();
      }
      if (c < 8) {
#pragma unroll
        for (int r = 0; r < 4; ++r) lds[L_GU + hw * 128 + 32 * g + 8 * r + c] = d[r];
      }
      asm volatile("s_waitcnt lgkmcnt(0)" ::: "memory");
      if (lane == 0) __hip_atomic_fetch_add((LAS unsigned*)&lds[L_SC + 4], 1u, __ATOMIC_RELAXED, __HIP_MEMORY_SCOPE_WORKGROUP);
    }
    const __amdgpu_buffer_rsrc_t rd_ = rsrc(p.wd, FF * DM * 2);
    const int voff = (64 * h + 16 * g) * 1024 + 256 * hw + 16 * c;
#pragma unroll
    for (int i = 0; i < 16; ++i) {
      W[i] = __builtin_amdgcn_raw_buffer_load_b128(rd_, voff, i * 1024, AUX_NT);
      if ((i % HD2) == HD2 - 1) asm volatile("s_waitcnt vmcnt(0)" ::: "memory");
    }
    while (__hip_atomic_load((LAS unsigned*)&lds[L_SC + 4], __ATOMIC_RELAXED, __HIP_MEMORY_SCOPE_WORKGROUP) < 4u) __builtin_amdgcn_s_sleep(4);
    asm volatile("" ::: "memory");
    float gt = 0.f, up = 0.f;
#pragma unroll
    for (int w = 0; w < 4; ++w) { gt += lds[L_GU + w * 128 + lane]; up += lds[L_GU + w * 128 + 64 + lane]; }
    gt = rnd_bf16(gt); up = rnd_bf16(up);
    const float si = rnd_bf16(gt * sigmoidf_(gt));
    lds[L_H + hw * 64 + lane] = __uint_as_float(pk_bf16(si * up, si * up));
    asm volatile("s_waitcnt lgkmcnt(0)" ::: "memory");
    u32x4 hp[4];
#pragma unroll
    for (int i = 0; i < 4; ++i) hp[i] = *reinterpret_cast<const u32x4*>(&lds[L_H + hw * 64 + 16 * g + 4 * i]);
#pragma unroll
    for (int i = 0; i < 16; ++i) dy = vm_step(W[i], hp[i >> 2][i & 3], msk, dy);
  }
  STAMP(2);
  u32x4 WB[8][4];
  {
    const __amdgpu_buffer_rsrc_t ro = rsrc(p.wo, NH * DH * DM * 2);
    const int voff = (128 * h + g) * 1024 + 256 * hw + 16 * c;
    float* st = &lds[L_ST + hw * 2048];
#pragma unroll
    for (int s = 0; s < 4; ++s) {
#pragma unroll
      for (int hf = 0; hf < 2; ++hf) {
#pragma unroll
        for (int i = 0; i < 4; ++i) W[i] = __builtin_amdgcn_raw_buffer_load_b128(ro, voff, (32 * s + 16 * hf + 4 * i) * 1024, AUX_NT);
#pragma unroll
        for (int i = 0; i < 4; ++i) *reinterpret_cast<u32x4*>(&st[(16 * hf + 4 * i + g) * 64 + 4 * c]) = W[i];
      }
      asm volatile("s_waitcnt lgkmcnt(0)" ::: "memory");
#pragma unroll
      for (int t = 0; t < 8; ++t) {
        const float* a0 = &st[(8 * g + (c >> 2)) * 64 + 8 * t + 2 * (c & 3)];
        const s16x4 u0 = __builtin_amdgcn_ds_read_tr16_b64_v4i16((LAS s16x4*)a0);
        const s16x4 u1 = __builtin_amdgcn_ds_read_tr16_b64_v4i16((LAS s16x4*)(a0 + 4 * 64));
        const u32x2 x0 = __builtin_bit_cast(u32x2, u0), x1 = __builtin_bit_cast(u32x2, u1);
        WB[t][s] = u32x4{x0[0], x0[1], x1[0], x1[1]};
      }
      asm volatile("s_waitcnt lgkmcnt(0)" ::: "memory");
    }
  }
  STAMP(3);
  wg_barrier();
  STAMP(4);
  {
    float mw[NSW], lw[NSW];
#pragma unroll
    for (int w = 0; w < NSW; ++w) { mw[w] = lds[L_MRG + w * 132 + 128]; lw[w] = lds[L_MRG + w * 132 + 129]; }
    float M = mw[0];
#pragma unroll
    for (int w = 1; w < NSW; ++w) M = fmaxf(M, mw[w]);
    float L = 0.f, o0 = 0.f, o1 = 0.f;
#pragma unroll
    for (int w = 0; w < NSW; ++w) {
      const float wt = __builtin_amdgcn_exp2f(mw[w] - M);
      const u32x2 ov = *reinterpret_cast<const u32x2*>(&lds[L_MRG + w * 132 + 32 * g + 2 * c]);
      L = fmaf(wt, lw[w], L); o0 = fmaf(wt, __uint_as_float(ov[0]), o0); o1 = fmaf(wt, __uint_as_float(ov[1]), o1);
    }
    const float inv = __builtin_amdgcn_rcpf(L);
    float* ob = &lds[L_H + hw * 64];
    ob[16 * g + c] = __uint_as_float(pk_bf16(o0 * inv, o1 * inv));
    asm volatile("s_waitcnt lgkmcnt(0)" ::: "memory");
    u32x4 oa[4];
#pragma unroll
    for (int s = 0; s < 4; ++s) oa[s] = *reinterpret_cast<const u32x4*>(&ob[16 * s + 4 * g]);
    f32x4 d[8];
#pragma unroll
    for (int t = 0; t < 8; ++t) d[t] = f32x4{0, 0, 0, 0};
#pragma unroll
    for (int s = 0; s < 4; ++s)
#pragma unroll
      for (int t = 0; t < 8; ++t) d[t] = mfma(oa[s], WB[t][s], d[t]);
    unsigned m1 = (g & 1) ? ~0u : 0u, m2 = (g & 2) ? ~0u : 0u;
    asm volatile("" : "+v"(m1), "+v"(m2));
    const float gate = lds[L_SC + 0], yc = lds[L_SC + 1] * lds[L_SC + 2];
    const unsigned tag = __float_as_uint(lds[L_SC + 3]);
    const __amdgpu_buffer_rsrc_t rin = rsrc(p.ws + WS_INBOX + (size_t)b * 32 * 4096, 32 * 4096);
#pragma unroll
    for (int pi = 0; pi < 2; ++pi) {
      const unsigned lo = (m1 & __float_as_uint(d[2 + pi][0])) | (~m1 & __float_as_uint(d[pi][0]));
      const unsigned hi = (m1 & __float_as_uint(d[6 + pi][0])) | (~m1 & __float_as_uint(d[4 + pi][0]));
      const float a0 = __uint_as_float((m2 & hi) | (~m2 & lo));
      const float a1 = dppf<ROR8>(a0);
      if (c < 8) {
        const float v0 = gate * a0 + yc * dy[2 * pi], v1 = gate * a1 + yc * dy[2 * pi + 1];
        const u32x4 gr = {__float_as_uint(v0), tag, __float_as_uint(v1), tag};
        __builtin_amdgcn_raw_buffer_store_b128(gr, rin, (8 * hw + 2 * g + pi) * 4096 + h * 128 + 16 * c, 0, AUX_SC1);
      }
    }
    STAMP(5);
    STAMP_FLUSH();
  }
}

extern "C" void kernel_launch(void* const* d_in, const int* in_sizes, int n_in, void* d_out, int out_size, void* d_ws, size_t ws_size,
                              hipStream_t stream) {
  Params p;
  p.xA = (const unsigned short*)d_in[0]; p.K = (const unsigned short*)d_in[1]; p.V = (const unsigned short*)d_in[2];
  p.wq = (const unsigned short*)d_in[3]; p.wo = (const unsigned short*)d_in[4];
  p.wr = (const unsigned short*)d_in[6]; p.wg = (const unsigned short*)d_in[7]; p.wu = (const unsigned short*)d_in[8];
  p.wd = (const unsigned short*)d_in[9]; p.gw = (const unsigned short*)d_in[10];
  p.out = (unsigned short*)d_out; p.ws = (unsigned char*)d_ws;
  fused_decode_kernel<<<NB * NH, NT, 0, stream>>>(p);
}
```

```cpp
#include <hip/hip_runtime.h>
#include <hip/hip_bf16.h>

typedef unsigned u32x4 __attribute__((ext_vector_type(4)));
typedef unsigned u32x2 __attribute__((ext_vector_type(2)));
typedef float f32x4 __attribute__((ext_vector_type(4)));
typedef short s16x4 __attribute__((ext_vector_type(4)));
typedef __bf16 bf16x2 __attribute__((ext_vector_type(2)));
typedef __bf16 bf16x8 __attribute__((ext_vector_type(8)));
#define GAS __attribute__((address_space(1)))
#define LAS __attribute__((address_space(3)))
typedef GAS unsigned gu32;

constexpr int NB = 8, NH = 32, SK = 4096, DH = 128, DM = 512, FF = 1792;
constexpr int NSW = 4;
constexpr int NT = (NSW + 4) * 64;
constexpr int TILES = SK / 16 / NSW;
constexpr int BURST = 3;
constexpr int HD = 8;
constexpr int HD2 = 4;
#define CODE_PF 13312
#define CODE_PF_NT 0
#define CODE_PF_WAVE 3
#define PACE_DT 117
#define PACE_T0 160
constexpr float C2 = 0.08838834764831845f * 1.4426950408889634f;
constexpr int RSRC3 = 0x00020000;
constexpr int AUX_NT = 2, AUX_SC1 = 16;
constexpr int WS_EPOCH = 0, WS_INBOX = 4096;
constexpr unsigned SPIN_CAP = 1u << 16;

struct Params {
  const unsigned short *K, *V, *xA, *wq, *wo, *wr, *wg, *wu, *wd, *gw;
  unsigned short* out;
  unsigned char* ws;
};

constexpr int L_QP = 0;
constexpr int L_MRG = L_QP + 512;
constexpr int L_GU = L_MRG + NSW * 132;
constexpr int L_H = L_GU + 512;
constexpr int L_SC = L_H + 256;
constexpr int L_ST = L_SC + 8;
constexpr int L_END = L_ST + 4 * 2048;

__device__ __forceinline__ float dot2(unsigned a, unsigned b, float c) {
  return __builtin_amdgcn_fdot2_f32_bf16(__builtin_bit_cast(bf16x2, a), __builtin_bit_cast(bf16x2, b), c, false);
}
__device__ __forceinline__ unsigned pk_bf16(float lo, float hi) {
  bf16x2 v = {(__bf16)lo, (__bf16)hi};
  return __builtin_bit_cast(unsigned, v);
}
__device__ __forceinline__ float rnd_bf16(float x) { return (float)(__bf16)x; }
__device__ __forceinline__ float bf_lo(unsigned u) { return __builtin_bit_cast(float, u << 16); }
__device__ __forceinline__ float bf_hi(unsigned u) { return __builtin_bit_cast(float, u & 0xffff0000u); }
template <int CTRL> __device__ __forceinline__ float dppf(float x) {
  return __builtin_bit_cast(float, __builtin_amdgcn_mov_dpp(__builtin_bit_cast(int, x), CTRL, 0xf, 0xf, true));
}
template <int CTRL> __device__ __forceinline__ unsigned dppu(unsigned x) {
  return (unsigned)__builtin_amdgcn_mov_dpp((int)x, CTRL, 0xf, 0xf, true);
}
constexpr int XOR1 = 0xB1, XOR2 = 0x4E, XOR7 = 0x141, ROR8 = 0x128, ROR4 = 0x124, BCAST = 0x150;
__device__ __forceinline__ float xrow_sum(float x) {
  auto s = __builtin_amdgcn_permlane16_swap(__float_as_uint(x), __float_as_uint(x), false, false);
  x = __uint_as_float(s[0]) + __uint_as_float(s[1]);
  auto t = __builtin_amdgcn_permlane32_swap(__float_as_uint(x), __float_as_uint(x), false, false);
  return __uint_as_float(t[0]) + __uint_as_float(t[1]);
}
__device__ __forceinline__ float xrow_max(float x) {
  auto s = __builtin_amdgcn_permlane16_swap(__float_as_uint(x), __float_as_uint(x), false, false);
  x = fmaxf(__uint_as_float(s[0]), __uint_as_float(s[1]));
  auto t = __builtin_amdgcn_permlane32_swap(__float_as_uint(x), __float_as_uint(x), false, false);
  return fmaxf(__uint_as_float(t[0]), __uint_as_float(t[1]));
}
__device__ __forceinline__ float wave_sum(float x) {
  x += dppf<XOR1>(x); x += dppf<XOR2>(x); x += dppf<XOR7>(x); x += dppf<ROR8>(x);
  return xrow_sum(x);
}
__device__ __forceinline__ f32x4 mfma(u32x4 a, u32x4 b, f32x4 c) {
  return __builtin_amdgcn_mfma_f32_16x16x32_bf16(__builtin_bit_cast(bf16x8, a), __builtin_bit_cast(bf16x8, b), c, 0, 0, 0);
}
__device__ __forceinline__ void wg_barrier() {
  asm volatile("s_waitcnt lgkmcnt(0)" ::: "memory");
  __builtin_amdgcn_s_barrier();
  asm volatile("" ::: "memory");
}
__device__ __forceinline__ float sigmoidf_(float x) { return 1.f / (1.f + __expf(-x)); }
__device__ __forceinline__ float safe_max(float m) { return m == -INFINITY ? 0.f : m; }
__device__ __forceinline__ __amdgpu_buffer_rsrc_t rsrc(const void* p, int bytes) {
  return __builtin_amdgcn_make_buffer_rsrc(const_cast<void*>(p), 0, bytes, RSRC3);
}
__device__ __forceinline__ f32x4 vm_step(u32x4 w, unsigned pp, const u32x4& msk, f32x4 d) {
  u32x4 bq = {pp & msk[0], pp & msk[1], pp & msk[2], pp & msk[3]};
  return mfma(w, bq, d);
}
__device__ __forceinline__ unsigned rep16(unsigned pair, int odd) {
  return odd ? __builtin_amdgcn_perm(pair, pair, 0x03020302u) : __builtin_amdgcn_perm(pair, pair, 0x01000100u);
}
__device__ __forceinline__ u32x4 ldw(const unsigned short* p) { return __builtin_nontemporal_load(reinterpret_cast<const u32x4*>(p)); }
#define PIN() __builtin_amdgcn_sched_barrier(0)

#define STAMP_FLUSH() do {} while (0)
#define STAMP(i) do {} while (0)

__global__ void __launch_bounds__(NT, NT / 256) fused_decode_kernel(Params p) {
  __shared__ __attribute__((aligned(16))) float lds[L_END];
  const int tid = threadIdx.x, lane = tid & 63, wave = __builtin_amdgcn_readfirstlane(tid >> 6);
  const int g = lane >> 4, c = lane & 15;
  const int bid = blockIdx.x, h = (bid & 7) * 4 + ((bid >> 3) & 3), b = bid >> 5;
  STAMP(0);
  const unsigned long long t_entry = __builtin_amdgcn_s_memrealtime();
  unsigned long long pc0;
  asm volatile("s_getpc_b64 %0" : "=s"(pc0));
  const unsigned short *Kp = p.K, *Vp = p.V;
  asm volatile("" : "+s"(Kp), "+s"(Vp));

  if (__builtin_expect(wave < NSW, 1)) {
    const size_t bh = (size_t)(b * NH + h) * SK * DH;
    const __amdgpu_buffer_rsrc_t rk = rsrc(Kp + bh, SK * DH * 2), rv = rsrc(Vp + bh, SK * DH * 2);
    const int voff = lane * 16;
    unsigned cpf[2] = {0u, 0u};
    static_assert(CODE_PF <= 16384, "two loads");
    if (wave == CODE_PF_WAVE) {
      const unsigned char* pc = (const unsigned char*)(pc0 & ~127ull);
#pragma unroll
      for (int i = 0; i < (CODE_PF + 8191) / 8192; ++i)
        if (i * 8192 + lane * 128 < CODE_PF) {
          cpf[i] = *(const GAS unsigned*)(pc + i * 8192 + lane * 128);
        }
    }
    auto ldk = [&](int i, int j) { return __builtin_amdgcn_raw_buffer_load_b128(rk, voff, (i * NSW + wave) * 4096 + j * 1024, AUX_NT); };
    auto ldv = [&](int i, int j) { return __builtin_amdgcn_raw_buffer_load_b128(rv, voff, (i * NSW + wave) * 4096 + j * 1024, AUX_NT); };
    u32x4 RK[4], RV[4], EK[BURST][4], EV[BURST][4];
#pragma unroll
    for (int j = 0; j < 4; ++j) RK[j] = ldk(BURST, j);
#pragma unroll
    for (int j = 0; j < 4; ++j) RV[j] = ldv(BURST, j);
#pragma unroll
    for (int e = 0; e < BURST; ++e) {
#pragma unroll
      for (int j = 0; j < 4; ++j) EK[e][j] = ldk(e, j);
#pragma unroll
      for (int j = 0; j < 4; ++j) EV[e][j] = ldv(e, j);
    }
    STAMP(7);
    wg_barrier();
    STAMP(1);
    asm volatile("" :: "v"(cpf[0]), "v"(cpf[1]));
    unsigned qp[4];
    {
      f32x4 q0 = {0, 0, 0, 0}, q1 = {0, 0, 0, 0};
#pragma unroll
      for (int w = 0; w < 4; ++w) {
        q0 += *reinterpret_cast<const f32x4*>(&lds[L_QP + w * 128 + 8 * c]);
        q1 += *reinterpret_cast<const f32x4*>(&lds[L_QP + w * 128 + 8 * c + 4]);
      }
      qp[0] = pk_bf16(q0[0], q0[1]); qp[1] = pk_bf16(q0[2], q0[3]); qp[2] = pk_bf16(q1[0], q1[1]); qp[3] = pk_bf16(q1[2], q1[3]);
    }
    float m = -INFINITY, ms = 0.f, l = 0.f, acc[8];
#pragma unroll
    for (int k = 0; k < 8; ++k) acc[k] = 0.f;
    const bool b8 = c & 8, b4 = c & 4;

    auto process = [&](u32x4 (&TK)[4], u32x4 (&TV)[4], bool refill, int inext) {
      float x[4];
#pragma unroll
      for (int j = 0; j < 4; ++j) {
        float d = dot2(TK[j][0], qp[0], 0.f);
        d = dot2(TK[j][1], qp[1], d); d = dot2(TK[j][2], qp[2], d); d = dot2(TK[j][3], qp[3], d);
        x[j] = d;
        if (refill) { TK[j] = ldk(inext, j); PIN(); }
      }
      const float a0 = x[0] + dppf<ROR8>(x[0]), a1 = x[1] + dppf<ROR8>(x[1]), a2 = x[2] + dppf<ROR8>(x[2]), a3 = x[3] + dppf<ROR8>(x[3]);
      const float y0 = b8 ? a2 : a0, y1 = b8 ? a3 : a1;
      const float z0 = y0 + dppf<XOR7>(y0), z1 = y1 + dppf<XOR7>(y1);
      float z = b4 ? z1 : z0;
      z += dppf<XOR2>(z); z += dppf<XOR1>(z);
      const float sc = z * C2;
      float pm = fmaxf(sc, dppf<ROR4>(sc)); pm = fmaxf(pm, dppf<ROR8>(pm));
      if (__any(pm > m)) {
        const float mn = fmaxf(m, pm), msn = safe_max(mn), al = __builtin_amdgcn_exp2f(m == -INFINITY ? -INFINITY : m - msn);
        l *= al;
#pragma unroll
        for (int k = 0; k < 8; ++k) acc[k] *= al;
        m = mn; ms = msn;
      }
      const float pe = __builtin_amdgcn_exp2f(sc - ms);
      l += pe;
      const unsigned plo = pk_bf16(pe, 0.f), phi = plo << 16;
#define PV(j) { const unsigned lo_ = dppu<BCAST + 4 * (j)>(plo), hi_ = dppu<BCAST + 4 * (j)>(phi); \
        _Pragma("unroll") for (int k = 0; k < 4; ++k) { acc[2 * k] = dot2(TV[j][k], lo_, acc[2 * k]); acc[2 * k + 1] = dot2(TV[j][k], hi_, acc[2 * k + 1]); } \
        if (refill) { TV[j] = ldv(inext, j); PIN(); } }
      PV(0) PV(1) PV(2) PV(3)
#undef PV
      asm volatile("" : "+v"(acc[0]), "+v"(acc[1]), "+v"(acc[2]), "+v"(acc[3]), "+v"(acc[4]), "+v"(acc[5]), "+v"(acc[6]), "+v"(acc[7]));
    };
    process(RK, RV, true, BURST + 1);
    STAMP(2);
#pragma unroll
    for (int e = 0; e < BURST; ++e) process(EK[e], EV[e], false, 0);
    unsigned long long tnext = t_entry + PACE_T0 + BURST * PACE_DT;
    for (int i = BURST + 1; i < TILES - 1; ++i) {
      tnext += PACE_DT;
      while ((long long)(__builtin_amdgcn_s_memrealtime() - tnext) < 0) __builtin_amdgcn_s_sleep(1);
      process(RK, RV, true, i + 1);
    }
    process(RK, RV, false, 0);
    STAMP(3);
    unsigned xfin = 0;
    if (wave == 0 && lane < 16) xfin = p.xA[b * DM + 16 * h + lane];
    {
      const float M = xrow_max(m), sc = __builtin_amdgcn_exp2f(m - safe_max(M));
      float ls = l * sc;
      ls += dppf<ROR4>(ls); ls += dppf<ROR8>(ls);
      ls = xrow_sum(ls);
      float o[8];
#pragma unroll
      for (int k = 0; k < 8; ++k) o[k] = xrow_sum(acc[k] * sc);
      float* rec = &lds[L_MRG + wave * 132];
      if (g == 0) {
        *reinterpret_cast<f32x4*>(rec + 8 * c) = f32x4{o[0], o[1], o[2], o[3]};
        *reinterpret_cast<f32x4*>(rec + 8 * c + 4) = f32x4{o[4], o[5], o[6], o[7]};
        if (c == 0) { rec[128] = M; rec[129] = ls; }
      }
    }
    wg_barrier();
    STAMP(4);
    if (wave != 0) { STAMP_FLUSH(); return; }
    const unsigned tag = __float_as_uint(lds[L_SC + 3]);
    const __amdgpu_buffer_rsrc_t rin = rsrc(p.ws + WS_INBOX + (size_t)(b * 32 + h) * 4096, 4096);
    u32x4 gv[4];
#pragma unroll
    for (int q = 0; q < 4; ++q) gv[q] = u32x4{0u, ~tag, 0u, ~tag};
    for (unsigned spins = 0;; ++spins) {
#pragma unroll
      for (int q = 0; q < 4; ++q)
        if (gv[q][1] != tag || gv[q][3] != tag) gv[q] = __builtin_amdgcn_raw_buffer_load_b128(rin, lane * 16 + q * 1024, 0, AUX_SC1);
      int missing = 0;
#pragma unroll
      for (int q = 0; q < 4; ++q) missing += __popcll(__ballot(gv[q][1] != tag || gv[q][3] != tag));
      if (missing == 0 || spins > SPIN_CAP) break;
      if (missing > 32) __builtin_amdgcn_s_sleep(16);
    }
    STAMP(5);
    float s0 = 0.f, s1 = 0.f;
#pragma unroll
    for (int q = 0; q < 4; ++q) { s0 += __uint_as_float(gv[q][0]); s1 += __uint_as_float(gv[q][2]); }
    s0 += dppf<ROR8>(s0); s1 += dppf<ROR8>(s1);
    s0 = xrow_sum(s0); s1 = xrow_sum(s1);
    if (lane < 16) {
      const float s = lane < 8 ? s0 : s1;
      const float xb = __builtin_bit_cast(float, xfin << 16);
      const float r = s + lds[L_SC + 1] * (xb + xb * 0.f);
      p.out[b * DM + 16 * h + lane] = __builtin_bit_cast(unsigned short, (__bf16)r);
    }
    STAMP(6);
    if (h == 0 && lane == 0) __hip_atomic_store((gu32*)(p.ws + WS_EPOCH + b * 256), tag, __ATOMIC_RELAXED, __HIP_MEMORY_SCOPE_AGENT);
    STAMP_FLUSH();
    return;
  }

  const int hw = wave - NSW;
  __builtin_amdgcn_s_setprio(3);
  u32x4 msk;
#pragma unroll
  for (int w = 0; w < 4; ++w) msk[w] = (c < 8 && (c >> 1) == w) ? ((c & 1) ? 0xffff0000u : 0x0000ffffu) : 0u;
  u32x4 W[16], xp[4];
  {
    const __amdgpu_buffer_rsrc_t rq = rsrc(p.wq, DM * NH * DH * 2);
    const u32x4* xs = reinterpret_cast<const u32x4*>(p.xA + b * DM + 128 * hw + 32 * g);
#pragma unroll
    for (int i = 0; i < 4; ++i) xp[i] = xs[i];
    const int voff = (128 * hw + 32 * g) * 8192 + h * 256 + 16 * c;
#pragma unroll
    for (int i = 0; i < HD; ++i) W[i] = __builtin_amdgcn_raw_buffer_load_b128(rq, voff, i * 8192, AUX_NT);
    if (hw == 0 && lane == 0) lds[L_SC + 4] = 0.f;
    f32x4 d = {0, 0, 0, 0};
#pragma unroll
    for (int i = 0; i < 32; ++i) {
      d = vm_step(W[i % HD], rep16(xp[i >> 3][(i >> 1) & 3], i & 1), msk, d);
      if (i + HD < 32) W[i % HD] = __builtin_amdgcn_raw_buffer_load_b128(rq, voff, (i + HD) * 8192, AUX_NT);
      PIN();
    }
    if (c < 8) {
#pragma unroll
      for (int r = 0; r < 4; ++r) lds[L_QP + hw * 128 + 32 * g + 8 * r + c] = d[r];
    }
  }
  STAMP(1);
  __builtin_amdgcn_s_setprio(0);
  wg_barrier();
  if (hw == 2 && lane == 0) {
    const unsigned epoch = __hip_atomic_load((gu32*)(p.ws + WS_EPOCH + b * 256), __ATOMIC_RELAXED, __HIP_MEMORY_SCOPE_AGENT);
    lds[L_SC + 3] = __uint_as_float(epoch + 1u);
  }
  if (hw == 3) {
    const u32x4 xa = *reinterpret_cast<const u32x4*>(p.xA + b * DM + 8 * lane), gw = *reinterpret_cast<const u32x4*>(p.gw + 8 * lane);
    const u32x4 wr = *reinterpret_cast<const u32x4*>(p.wr + 8 * lane);
    float sg = 0.f, sr = 0.f;
#pragma unroll
    for (int k = 0; k < 4; ++k) {
      sg += rnd_bf16(bf_lo(xa[k]) * bf_lo(gw[k])) + rnd_bf16(bf_hi(xa[k]) * bf_hi(gw[k]));
      sr = dot2(xa[k], wr[k], sr);
    }
    sg = wave_sum(sg); sr = wave_sum(sr);
    const float gate = rnd_bf16(sigmoidf_(rnd_bf16(sg)));
    const float rs = rnd_bf16(sigmoidf_(rnd_bf16(sr)));
    if (lane == 0) { lds[L_SC + 0] = gate; lds[L_SC + 1] = rnd_bf16(1.f - gate); lds[L_SC + 2] = rs; }
  }
  f32x4 dy = {0, 0, 0, 0};
  if (h < FF / 64) {
    {
      f32x4 d = {0, 0, 0, 0};
      const unsigned short* wbase = (c < 8 ? p.wg : p.wu) + 64 * h + 8 * (c & 7) + (size_t)(128 * hw + 32 * g) * FF;
#pragma unroll
      for (int i = 0; i < HD2; ++i) W[i] = ldw(wbase + (size_t)i * FF);
#pragma unroll
      for (int i = 0; i < 32; ++i) {
        d = vm_step(W[i % HD2], rep16(xp[i >> 3][(i >> 1) & 3], i & 1), msk, d);
        if (i + HD2 < 32) W[i % HD2] = ldw(wbase + (size_t)(i + HD2) * FF);
        PIN();
      }
      if (c < 8) {
#pragma unroll
        for (int r = 0; r < 4; ++r) lds[L_GU + hw * 128 + 32 * g + 8 * r + c] = d[r];
      }
      asm volatile("s_waitcnt lgkmcnt(0)" ::: "memory");
      if (lane == 0) __hip_atomic_fetch_add((LAS unsigned*)&lds[L_SC + 4], 1u, __ATOMIC_RELAXED, __HIP_MEMORY_SCOPE_WORKGROUP);
    }
    const __amdgpu_buffer_rsrc_t rd_ = rsrc(p.wd, FF * DM * 2);
    const int voff = (64 * h + 16 * g) * 1024 + 256 * hw + 16 * c;
#pragma unroll
    for (int i = 0; i < 16; ++i) {
      W[i] = __builtin_amdgcn_raw_buffer_load_b128(rd_, voff, i * 1024, AUX_NT);
      if ((i % HD2) == HD2 - 1) asm volatile("s_waitcnt vmcnt(0)" ::: "memory");
    }
    while (__hip_atomic_load((LAS unsigned*)&lds[L_SC + 4], __ATOMIC_RELAXED, __HIP_MEMORY_SCOPE_WORKGROUP) < 4u) __builtin_amdgcn_s_sleep(4);
    asm volatile("" ::: "memory");
    float gt = 0.f, up = 0.f;
#pragma unroll
    for (int w = 0; w < 4; ++w) { gt += lds[L_GU + w * 128 + lane]; up += lds[L_GU + w * 128 + 64 + lane]; }
    gt = rnd_bf16(gt); up = rnd_bf16(up);
    const float si = rnd_bf16(gt * sigmoidf_(gt));
    lds[L_H + hw * 64 + lane] = __uint_as_float(pk_bf16(si * up, si * up));
    asm volatile("s_waitcnt lgkmcnt(0)" ::: "memory");
    u32x4 hp[4];
#pragma unroll
    for (int i = 0; i < 4; ++i) hp[i] = *reinterpret_cast<const u32x4*>(&lds[L_H + hw * 64 + 16 * g + 4 * i]);
#pragma unroll
    for (int i = 0; i < 16; ++i) dy = vm_step(W[i], hp[i >> 2][i & 3], msk, dy);
  }
  STAMP(2);
  u32x4 WB[8][4];
  {
    const __amdgpu_buffer_rsrc_t ro = rsrc(p.wo, NH * DH * DM * 2);
    const int voff = (128 * h + g) * 1024 + 256 * hw + 16 * c;
    float* st = &lds[L_ST + hw * 2048];
#pragma unroll
    for (int s = 0; s < 4; ++s) {
#pragma unroll
      for (int hf = 0; hf < 2; ++hf) {
#pragma unroll
        for (int i = 0; i < 4; ++i) W[i] = __builtin_amdgcn_raw_buffer_load_b128(ro, voff, (32 * s + 16 * hf + 4 * i) * 1024, AUX_NT);
#pragma unroll
        for (int i = 0; i < 4; ++i) *reinterpret_cast<u32x4*>(&st[(16 * hf + 4 * i + g) * 64 + 4 * c]) = W[i];
      }
      asm volatile("s_waitcnt lgkmcnt(0)" ::: "memory");
#pragma unroll
      for (int t = 0; t < 8; ++t) {
        const float* a0 = &st[(8 * g + (c >> 2)) * 64 + 8 * t + 2 * (c & 3)];
        const s16x4 u0 = __builtin_amdgcn_ds_read_tr16_b64_v4i16((LAS s16x4*)a0);
        const s16x4 u1 = __builtin_amdgcn_ds_read_tr16_b64_v4i16((LAS s16x4*)(a0 + 4 * 64));
        const u32x2 x0 = __builtin_bit_cast(u32x2, u0), x1 = __builtin_bit_cast(u32x2, u1);
        WB[t][s] = u32x4{x0[0], x0[1], x1[0], x1[1]};
      }
      asm volatile("s_waitcnt lgkmcnt(0)" ::: "memory");
    }
  }
  STAMP(3);
  wg_barrier();
  STAMP(4);
  {
    float mw[NSW], lw[NSW];
#pragma unroll
    for (int w = 0; w < NSW; ++w) { mw[w] = lds[L_MRG + w * 132 + 128]; lw[w] = lds[L_MRG + w * 132 + 129]; }
    float M = mw[0];
#pragma unroll
    for (int w = 1; w < NSW; ++w) M = fmaxf(M, mw[w]);
    float L = 0.f, o0 = 0.f, o1 = 0.f;
#pragma unroll
    for (int w = 0; w < NSW; ++w) {
      const float wt = __builtin_amdgcn_exp2f(mw[w] - M);
      const u32x2 ov = *reinterpret_cast<const u32x2*>(&lds[L_MRG + w * 132 + 32 * g + 2 * c]);
      L = fmaf(wt, lw[w], L); o0 = fmaf(wt, __uint_as_float(ov[0]), o0); o1 = fmaf(wt, __uint_as_float(ov[1]), o1);
    }
    const float inv = __builtin_amdgcn_rcpf(L);
    float* ob = &lds[L_H + hw * 64];
    ob[16 * g + c] = __uint_as_float(pk_bf16(o0 * inv, o1 * inv));
    asm volatile("s_waitcnt lgkmcnt(0)" ::: "memory");
    u32x4 oa[4];
#pragma unroll
    for (int s = 0; s < 4; ++s) oa[s] = *reinterpret_cast<const u32x4*>(&ob[16 * s + 4 * g]);
    f32x4 d[8];
#pragma unroll
    for (int t = 0; t < 8; ++t) d[t] = f32x4{0, 0, 0, 0};
#pragma unroll
    for (int s = 0; s < 4; ++s)
#pragma unroll
      for (int t = 0; t < 8; ++t) d[t] = mfma(oa[s], WB[t][s], d[t]);
    unsigned m1 = (g & 1) ? ~0u : 0u, m2 = (g & 2) ? ~0u : 0u;
    asm volatile("" : "+v"(m1), "+v"(m2));
    const float gate = lds[L_SC + 0], yc = lds[L_SC + 1] * lds[L_SC + 2];
    const unsigned tag = __float_as_uint(lds[L_SC + 3]);
    const __amdgpu_buffer_rsrc_t rin = rsrc(p.ws + WS_INBOX + (size_t)b * 32 * 4096, 32 * 4096);
#pragma unroll
    for (int pi = 0; pi < 2; ++pi) {
      const unsigned lo = (m1 & __float_as_uint(d[2 + pi][0])) | (~m1 & __float_as_uint(d[pi][0]));
      const unsigned hi = (m1 & __float_as_uint(d[6 + pi][0])) | (~m1 & __float_as_uint(d[4 + pi][0]));
      const float a0 = __uint_as_float((m2 & hi) | (~m2 & lo));
      const float a1 = dppf<ROR8>(a0);
      if (c < 8) {
        const float v0 = gate * a0 + yc * dy[2 * pi], v1 = gate * a1 + yc * dy[2 * pi + 1];
        const u32x4 gr = {__float_as_uint(v0), tag, __float_as_uint(v1), tag};
        __builtin_amdgcn_raw_buffer_store_b128(gr, rin, (8 * hw + 2 * g + pi) * 4096 + h * 128 + 16 * c, 0, AUX_SC1);
      }
    }
    STAMP(5);
    STAMP_FLUSH();
  }
}

extern "C" void kernel_launch(void* const* d_in, const int* in_sizes, int n_in, void* d_out, int out_size, void* d_ws, size_t ws_size,
                              hipStream_t stream) {
  Params p;
  p.xA = (const unsigned short*)d_in[0]; p.K = (const unsigned short*)d_in[1]; p.V = (const unsigned short*)d_in[2];
  p.wq = (const unsigned short*)d_in[3]; p.wo = (const unsigned short*)d_in[4];
  p.wr = (const unsigned short*)d_in[6]; p.wg = (const unsigned short*)d_in[7]; p.wu = (const unsigned short*)d_in[8];
  p.wd = (const unsigned short*)d_in[9]; p.gw = (const unsigned short*)d_in[10];
  p.out = (unsigned short*)d_out; p.ws = (unsigned char*)d_ws;
  fused_decode_kernel<<<NB * NH, NT, 0, stream>>>(p);
}
```

```cpp
#include <hip/hip_runtime.h>
#include <hip/hip_bf16.h>

typedef unsigned u32x4 __attribute__((ext_vector_type(4)));
typedef unsigned u32x2 __attribute__((ext_vector_type(2)));
typedef float f32x4 __attribute__((ext_vector_type(4)));
typedef short s16x4 __attribute__((ext_vector_type(4)));
typedef __bf16 bf16x2 __attribute__((ext_vector_type(2)));
typedef __bf16 bf16x8 __attribute__((ext_vector_type(8)));
#define GAS __attribute__((address_space(1)))
#define LAS __attribute__((address_space(3)))
typedef GAS unsigned gu32;

constexpr int NB = 8, NH = 32, SK = 4096, DH = 128, DM = 512, FF = 1792;
constexpr int NSW = 4;
constexpr int NT = (NSW + 4) * 64;
constexpr int TILES = SK / 16 / NSW;
constexpr int BURST = 3;
constexpr int HD = 8;
constexpr int HD2 = 4;
#define ADAPT 1
#define ADAPT_M 130
#define ADAPT_Q 248
constexpr int PA = 8, PB = 22, PC = 30, T_READ = 3300;
#define CODE_PF 13312
#define CODE_PF_NT 0
#define CODE_PF_WAVE 3
#define PACE_DT 117
#define PACE_T0 160
constexpr float C2 = 0.08838834764831845f * 1.4426950408889634f;
constexpr int RSRC3 = 0x00020000;
constexpr int AUX_NT = 2, AUX_SC1 = 16;
constexpr int WS_EPOCH = 0, WS_INBOX = 4096, WS_PACE = WS_INBOX + (1 << 20);
constexpr unsigned SPIN_CAP = 1u << 16;

struct Params {
  const unsigned short *K, *V, *xA, *wq, *wo, *wr, *wg, *wu, *wd, *gw;
  unsigned short* out;
  unsigned char* ws;
};

constexpr int L_QP = 0;
constexpr int L_MRG = L_QP + 512;
constexpr int L_GU = L_MRG + NSW * 132;
constexpr int L_H = L_GU + 512;
constexpr int L_SC = L_H + 256;
constexpr int L_PC = L_SC + 8;
constexpr int L_ST = L_PC + 4;
constexpr int L_END = L_ST + 4 * 2048;

__device__ __forceinline__ float dot2(unsigned a, unsigned b, float c) {
  return __builtin_amdgcn_fdot2_f32_bf16(__builtin_bit_cast(bf16x2, a), __builtin_bit_cast(bf16x2, b), c, false);
}
__device__ __forceinline__ unsigned pk_bf16(float lo, float hi) {
  bf16x2 v = {(__bf16)lo, (__bf16)hi};
  return __builtin_bit_cast(unsigned, v);
}
__device__ __forceinline__ float rnd_bf16(float x) { return (float)(__bf16)x; }
__device__ __forceinline__ float bf_lo(unsigned u) { return __builtin_bit_cast(float, u << 16); }
__device__ __forceinline__ float bf_hi(unsigned u) { return __builtin_bit_cast(float, u & 0xffff0000u); }
template <int CTRL> __device__ __forceinline__ float dppf(float x) {
  return __builtin_bit_cast(float, __builtin_amdgcn_mov_dpp(__builtin_bit_cast(int, x), CTRL, 0xf, 0xf, true));
}
template <int CTRL> __device__ __forceinline__ unsigned dppu(unsigned x) {
  return (unsigned)__builtin_amdgcn_mov_dpp((int)x, CTRL, 0xf, 0xf, true);
}
constexpr int XOR1 = 0xB1, XOR2 = 0x4E, XOR7 = 0x141, ROR8 = 0x128, ROR4 = 0x124, BCAST = 0x150;
__device__ __forceinline__ float xrow_sum(float x) {
  auto s = __builtin_amdgcn_permlane16_swap(__float_as_uint(x), __float_as_uint(x), false, false);
  x = __uint_as_float(s[0]) + __uint_as_float(s[1]);
  auto t = __builtin_amdgcn_permlane32_swap(__float_as_uint(x), __float_as_uint(x), false, false);
  return __uint_as_float(t[0]) + __uint_as_float(t[1]);
}
__device__ __forceinline__ float xrow_max(float x) {
  auto s = __builtin_amdgcn_permlane16_swap(__float_as_uint(x), __float_as_uint(x), false, false);
  x = fmaxf(__uint_as_float(s[0]), __uint_as_float(s[1]));
  auto t = __builtin_amdgcn_permlane32_swap(__float_as_uint(x), __float_as_uint(x), false, false);
  return fmaxf(__uint_as_float(t[0]), __uint_as_float(t[1]));
}
__device__ __forceinline__ float wave_sum(float x) {
  x += dppf<XOR1>(x); x += dppf<XOR2>(x); x += dppf<XOR7>(x); x += dppf<ROR8>(x);
  return xrow_sum(x);
}
__device__ __forceinline__ f32x4 mfma(u32x4 a, u32x4 b, f32x4 c) {
  return __builtin_amdgcn_mfma_f32_16x16x32_bf16(__builtin_bit_cast(bf16x8, a), __builtin_bit_cast(bf16x8, b), c, 0, 0, 0);
}
__device__ __forceinline__ void wg_barrier() {
  asm volatile("s_waitcnt lgkmcnt(0)" ::: "memory");
  __builtin_amdgcn_s_barrier();
  asm volatile("" ::: "memory");
}
__device__ __forceinline__ float sigmoidf_(float x) { return 1.f / (1.f + __expf(-x)); }
__device__ __forceinline__ float safe_max(float m) { return m == -INFINITY ? 0.f : m; }
__device__ __forceinline__ __amdgpu_buffer_rsrc_t rsrc(const void* p, int bytes) {
  return __builtin_amdgcn_make_buffer_rsrc(const_cast<void*>(p), 0, bytes, RSRC3);
}
__device__ __forceinline__ f32x4 vm_step(u32x4 w, unsigned pp, const u32x4& msk, f32x4 d) {
  u32x4 bq = {pp & msk[0], pp & msk[1], pp & msk[2], pp & msk[3]};
  return mfma(w, bq, d);
}
__device__ __forceinline__ unsigned rep16(unsigned pair, int odd) {
  return odd ? __builtin_amdgcn_perm(pair, pair, 0x03020302u) : __builtin_amdgcn_perm(pair, pair, 0x01000100u);
}
__device__ __forceinline__ u32x4 ldw(const unsigned short* p) { return __builtin_nontemporal_load(reinterpret_cast<const u32x4*>(p)); }
#define PIN() __builtin_amdgcn_sched_barrier(0)

#define STAMP_FLUSH() do {} while (0)
#define STAMP(i) do {} while (0)

__global__ void __launch_bounds__(NT, NT / 256) fused_decode_kernel(Params p) {
  __shared__ __attribute__((aligned(16))) float lds[L_END];
  const int tid = threadIdx.x, lane = tid & 63, wave = __builtin_amdgcn_readfirstlane(tid >> 6);
  const int g = lane >> 4, c = lane & 15;
  const int bid = blockIdx.x, h = (bid & 7) * 4 + ((bid >> 3) & 3), b = bid >> 5;
  STAMP(0);
  const unsigned long long t_entry = __builtin_amdgcn_s_memrealtime();
  unsigned long long pc0;
  asm volatile("s_getpc_b64 %0" : "=s"(pc0));
  const unsigned short *Kp = p.K, *Vp = p.V;
  asm volatile("" : "+s"(Kp), "+s"(Vp));

  if (__builtin_expect(wave < NSW, 1)) {
    const size_t bh = (size_t)(b * NH + h) * SK * DH;
    const __amdgpu_buffer_rsrc_t rk = rsrc(Kp + bh, SK * DH * 2), rv = rsrc(Vp + bh, SK * DH * 2);
    const int voff = lane * 16;
    unsigned cpf[2] = {0u, 0u};
    static_assert(CODE_PF <= 16384, "two loads");
    if (wave == CODE_PF_WAVE) {
      const unsigned char* pc = (const unsigned char*)(pc0 & ~127ull);
#pragma unroll
      for (int i = 0; i < (CODE_PF + 8191) / 8192; ++i)
        if (i * 8192 + lane * 128 < CODE_PF) {
          cpf[i] = *(const GAS unsigned*)(pc + i * 8192 + lane * 128);
        }
    }
    auto ldk = [&](int i, int j) { return __builtin_amdgcn_raw_buffer_load_b128(rk, voff, (i * NSW + wave) * 4096 + j * 1024, AUX_NT); };
    auto ldv = [&](int i, int j) { return __builtin_amdgcn_raw_buffer_load_b128(rv, voff, (i * NSW + wave) * 4096 + j * 1024, AUX_NT); };
    u32x4 RK[4], RV[4], EK[BURST][4], EV[BURST][4];
#pragma unroll
    for (int j = 0; j < 4; ++j) RK[j] = ldk(BURST, j);
#pragma unroll
    for (int j = 0; j < 4; ++j) RV[j] = ldv(BURST, j);
#pragma unroll
    for (int e = 0; e < BURST; ++e) {
#pragma unroll
      for (int j = 0; j < 4; ++j) EK[e][j] = ldk(e, j);
#pragma unroll
      for (int j = 0; j < 4; ++j) EV[e][j] = ldv(e, j);
    }
    STAMP(7);
    wg_barrier();
    STAMP(1);
    asm volatile("" :: "v"(cpf[0]), "v"(cpf[1]));
    unsigned qp[4];
    {
      f32x4 q0 = {0, 0, 0, 0}, q1 = {0, 0, 0, 0};
#pragma unroll
      for (int w = 0; w < 4; ++w) {
        q0 += *reinterpret_cast<const f32x4*>(&lds[L_QP + w * 128 + 8 * c]);
        q1 += *reinterpret_cast<const f32x4*>(&lds[L_QP + w * 128 + 8 * c + 4]);
      }
      qp[0] = pk_bf16(q0[0], q0[1]); qp[1] = pk_bf16(q0[2], q0[3]); qp[2] = pk_bf16(q1[0], q1[1]); qp[3] = pk_bf16(q1[2], q1[3]);
    }
    float m = -INFINITY, ms = 0.f, l = 0.f, acc[8];
#pragma unroll
    for (int k = 0; k < 8; ++k) acc[k] = 0.f;
    const bool b8 = c & 8, b4 = c & 4;

    auto process = [&](u32x4 (&TK)[4], u32x4 (&TV)[4], bool refill, int inext) {
      float x[4];
#pragma unroll
      for (int j = 0; j < 4; ++j) {
        float d = dot2(TK[j][0], qp[0], 0.f);
        d = dot2(TK[j][1], qp[1], d); d = dot2(TK[j][2], qp[2], d); d = dot2(TK[j][3], qp[3], d);
        x[j] = d;
        if (refill) { TK[j] = ldk(inext, j); PIN(); }
      }
      const float a0 = x[0] + dppf<ROR8>(x[0]), a1 = x[1] + dppf<ROR8>(x[1]), a2 = x[2] + dppf<ROR8>(x[2]), a3 = x[3] + dppf<ROR8>(x[3]);
      const float y0 = b8 ? a2 : a0, y1 = b8 ? a3 : a1;
      const float z0 = y0 + dppf<XOR7>(y0), z1 = y1 + dppf<XOR7>(y1);
      float z = b4 ? z1 : z0;
      z += dppf<XOR2>(z); z += dppf<XOR1>(z);
      const float sc = z * C2;
      float pm = fmaxf(sc, dppf<ROR4>(sc)); pm = fmaxf(pm, dppf<ROR8>(pm));
      if (__any(pm > m)) {
        const float mn = fmaxf(m, pm), msn = safe_max(mn), al = __builtin_amdgcn_exp2f(m == -INFINITY ? -INFINITY : m - msn);
        l *= al;
#pragma unroll
        for (int k = 0; k < 8; ++k) acc[k] *= al;
        m = mn; ms = msn;
      }
      const float pe = __builtin_amdgcn_exp2f(sc - ms);
      l += pe;
      const unsigned plo = pk_bf16(pe, 0.f), phi = plo << 16;
#define PV(j) { const unsigned lo_ = dppu<BCAST + 4 * (j)>(plo), hi_ = dppu<BCAST + 4 * (j)>(phi); \
        _Pragma("unroll") for (int k = 0; k < 4; ++k) { acc[2 * k] = dot2(TV[j][k], lo_, acc[2 * k]); acc[2 * k + 1] = dot2(TV[j][k], hi_, acc[2 * k + 1]); } \
        if (refill) { TV[j] = ldv(inext, j); PIN(); } }
      PV(0) PV(1) PV(2) PV(3)
#undef PV
      asm volatile("" : "+v"(acc[0]), "+v"(acc[1]), "+v"(acc[2]), "+v"(acc[3]), "+v"(acc[4]), "+v"(acc[5]), "+v"(acc[6]), "+v"(acc[7]));
    };
    process(RK, RV, true, BURST + 1);
    STAMP(2);
#pragma unroll
    for (int e = 0; e < BURST; ++e) process(EK[e], EV[e], false, 0);
    unsigned long long tnext = t_entry + PACE_T0 + BURST * PACE_DT;
    unsigned dt = PACE_DT;
    for (int i = BURST + 1; i < TILES - 1; ++i) {
      tnext += dt;
      unsigned long long now = __builtin_amdgcn_s_memrealtime();
      while ((long long)(now - tnext) < 0) { __builtin_amdgcn_s_sleep(1); now = __builtin_amdgcn_s_memrealtime(); }
      if (wave == 0 && (i == PA || i == PB)) { if (lane == 0) lds[L_PC + (i == PB)] = __uint_as_float((unsigned)(now - t_entry)); }
      if (i == PC) {
        const unsigned nb = __builtin_amdgcn_readfirstlane(__float_as_uint(lds[L_PC + 2])), ns = __builtin_amdgcn_readfirstlane(__float_as_uint(lds[L_PC + 3]));
        if (ns != 0) { tnext = t_entry + nb; dt = ns; }
      }
      process(RK, RV, true, i + 1);
    }
    process(RK, RV, false, 0);
    STAMP(3);
    unsigned xfin = 0;
    if (wave == 0 && lane < 16) xfin = p.xA[b * DM + 16 * h + lane];
    {
      const float M = xrow_max(m), sc = __builtin_amdgcn_exp2f(m - safe_max(M));
      float ls = l * sc;
      ls += dppf<ROR4>(ls); ls += dppf<ROR8>(ls);
      ls = xrow_sum(ls);
      float o[8];
#pragma unroll
      for (int k = 0; k < 8; ++k) o[k] = xrow_sum(acc[k] * sc);
      float* rec = &lds[L_MRG + wave * 132];
      if (g == 0) {
        *reinterpret_cast<f32x4*>(rec + 8 * c) = f32x4{o[0], o[1], o[2], o[3]};
        *reinterpret_cast<f32x4*>(rec + 8 * c + 4) = f32x4{o[4], o[5], o[6], o[7]};
        if (c == 0) { rec[128] = M; rec[129] = ls; }
      }
    }
    wg_barrier();
    STAMP(4);
    if (wave != 0) { STAMP_FLUSH(); return; }
    const unsigned tag = __float_as_uint(lds[L_SC + 3]);
    const __amdgpu_buffer_rsrc_t rin = rsrc(p.ws + WS_INBOX + (size_t)(b * 32 + h) * 4096, 4096);
    u32x4 gv[4];
#pragma unroll
    for (int q = 0; q < 4; ++q) gv[q] = u32x4{0u, ~tag, 0u, ~tag};
    for (unsigned spins = 0;; ++spins) {
#pragma unroll
      for (int q = 0; q < 4; ++q)
        if (gv[q][1] != tag || gv[q][3] != tag) gv[q] = __builtin_amdgcn_raw_buffer_load_b128(rin, lane * 16 + q * 1024, 0, AUX_SC1);
      int missing = 0;
#pragma unroll
      for (int q = 0; q < 4; ++q) missing += __popcll(__ballot(gv[q][1] != tag || gv[q][3] != tag));
      if (missing == 0 || spins > SPIN_CAP) break;
      if (missing > 32) __builtin_amdgcn_s_sleep(16);
    }
    STAMP(5);
    float s0 = 0.f, s1 = 0.f;
#pragma unroll
    for (int q = 0; q < 4; ++q) { s0 += __uint_as_float(gv[q][0]); s1 += __uint_as_float(gv[q][2]); }
    s0 += dppf<ROR8>(s0); s1 += dppf<ROR8>(s1);
    s0 = xrow_sum(s0); s1 = xrow_sum(s1);
    if (lane < 16) {
      const float s = lane < 8 ? s0 : s1;
      const float xb = __builtin_bit_cast(float, xfin << 16);
      const float r = s + lds[L_SC + 1] * (xb + xb * 0.f);
      p.out[b * DM + 16 * h + lane] = __builtin_bit_cast(unsigned short, (__bf16)r);
    }
    STAMP(6);
    if (h == 0 && lane == 0) __hip_atomic_store((gu32*)(p.ws + WS_EPOCH + b * 256), tag, __ATOMIC_RELAXED, __HIP_MEMORY_SCOPE_AGENT);
    STAMP_FLUSH();
    return;
  }

  const int hw = wave - NSW;
  __builtin_amdgcn_s_setprio(3);
  u32x4 msk;
#pragma unroll
  for (int w = 0; w < 4; ++w) msk[w] = (c < 8 && (c >> 1) == w) ? ((c & 1) ? 0xffff0000u : 0x0000ffffu) : 0u;
  u32x4 W[16], xp[4];
  {
    const __amdgpu_buffer_rsrc_t rq = rsrc(p.wq, DM * NH * DH * 2);
    const u32x4* xs = reinterpret_cast<const u32x4*>(p.xA + b * DM + 128 * hw + 32 * g);
#pragma unroll
    for (int i = 0; i < 4; ++i) xp[i] = xs[i];
    const int voff = (128 * hw + 32 * g) * 8192 + h * 256 + 16 * c;
#pragma unroll
    for (int i = 0; i < HD; ++i) W[i] = __builtin_amdgcn_raw_buffer_load_b128(rq, voff, i * 8192, AUX_NT);
    if (hw == 0 && lane == 0) lds[L_SC + 4] = 0.f;
    if (hw == 0 && lane < 4) lds[L_PC + lane] = 0.f;
    f32x4 d = {0, 0, 0, 0};
#pragma unroll
    for (int i = 0; i < 32; ++i) {
      d = vm_step(W[i % HD], rep16(xp[i >> 3][(i >> 1) & 3], i & 1), msk, d);
      if (i + HD < 32) W[i % HD] = __builtin_amdgcn_raw_buffer_load_b128(rq, voff, (i + HD) * 8192, AUX_NT);
      PIN();
    }
    if (c < 8) {
#pragma unroll
      for (int r = 0; r < 4; ++r) lds[L_QP + hw * 128 + 32 * g + 8 * r + c] = d[r];
    }
  }
  STAMP(1);
  __builtin_amdgcn_s_setprio(0);
  wg_barrier();
  if (hw == 2 && lane == 0) {
    const unsigned epoch = __hip_atomic_load((gu32*)(p.ws + WS_EPOCH + b * 256), __ATOMIC_RELAXED, __HIP_MEMORY_SCOPE_AGENT);
    lds[L_SC + 3] = __uint_as_float(epoch + 1u);
  }
  if (hw == 3) {
    const u32x4 xa = *reinterpret_cast<const u32x4*>(p.xA + b * DM + 8 * lane), gw = *reinterpret_cast<const u32x4*>(p.gw + 8 * lane);
    const u32x4 wr = *reinterpret_cast<const u32x4*>(p.wr + 8 * lane);
    float sg = 0.f, sr = 0.f;
#pragma unroll
    for (int k = 0; k < 4; ++k) {
      sg += rnd_bf16(bf_lo(xa[k]) * bf_lo(gw[k])) + rnd_bf16(bf_hi(xa[k]) * bf_hi(gw[k]));
      sr = dot2(xa[k], wr[k], sr);
    }
    sg = wave_sum(sg); sr = wave_sum(sr);
    const float gate = rnd_bf16(sigmoidf_(rnd_bf16(sg)));
    const float rs = rnd_bf16(sigmoidf_(rnd_bf16(sr)));
    if (lane == 0) { lds[L_SC + 0] = gate; lds[L_SC + 1] = rnd_bf16(1.f - gate); lds[L_SC + 2] = rs; }
  }
  f32x4 dy = {0, 0, 0, 0};
  if (h < FF / 64) {
    {
      f32x4 d = {0, 0, 0, 0};
      const unsigned short* wbase = (c < 8 ? p.wg : p.wu) + 64 * h + 8 * (c & 7) + (size_t)(128 * hw + 32 * g) * FF;
#pragma unroll
      for (int i = 0; i < HD2; ++i) W[i] = ldw(wbase + (size_t)i * FF);
#pragma unroll
      for (int i = 0; i < 32; ++i) {
        d = vm_step(W[i % HD2], rep16(xp[i >> 3][(i >> 1) & 3], i & 1), msk, d);
        if (i + HD2 < 32) W[i % HD2] = ldw(wbase + (size_t)(i + HD2) * FF);
        PIN();
      }
      if (c < 8) {
#pragma unroll
        for (int r = 0; r < 4; ++r) lds[L_GU + hw * 128 + 32 * g + 8 * r + c] = d[r];
      }
      asm volatile("s_waitcnt lgkmcnt(0)" ::: "memory");
      if (lane == 0) __hip_atomic_fetch_add((LAS unsigned*)&lds[L_SC + 4], 1u, __ATOMIC_RELAXED, __HIP_MEMORY_SCOPE_WORKGROUP);
    }
    const __amdgpu_buffer_rsrc_t rd_ = rsrc(p.wd, FF * DM * 2);
    const int voff = (64 * h + 16 * g) * 1024 + 256 * hw + 16 * c;
#pragma unroll
    for (int i = 0; i < 16; ++i) {
      W[i] = __builtin_amdgcn_raw_buffer_load_b128(rd_, voff, i * 1024, AUX_NT);
      if ((i % HD2) == HD2 - 1) asm volatile("s_waitcnt vmcnt(0)" ::: "memory");
    }
    while (__hip_atomic_load((LAS unsigned*)&lds[L_SC + 4], __ATOMIC_RELAXED, __HIP_MEMORY_SCOPE_WORKGROUP) < 4u) __builtin_amdgcn_s_sleep(4);
    asm volatile("" ::: "memory");
    float gt = 0.f, up = 0.f;
#pragma unroll
    for (int w = 0; w < 4; ++w) { gt += lds[L_GU + w * 128 + lane]; up += lds[L_GU + w * 128 + 64 + lane]; }
    gt = rnd_bf16(gt); up = rnd_bf16(up);
    const float si = rnd_bf16(gt * sigmoidf_(gt));
    lds[L_H + hw * 64 + lane] = __uint_as_float(pk_bf16(si * up, si * up));
    asm volatile("s_waitcnt lgkmcnt(0)" ::: "memory");
    u32x4 hp[4];
#pragma unroll
    for (int i = 0; i < 4; ++i) hp[i] = *reinterpret_cast<const u32x4*>(&lds[L_H + hw * 64 + 16 * g + 4 * i]);
#pragma unroll
    for (int i = 0; i < 16; ++i) dy = vm_step(W[i], hp[i >> 2][i & 3], msk, dy);
  }
  if (hw == 2) {
    while (__hip_atomic_load((LAS unsigned*)&lds[L_PC + 1], __ATOMIC_RELAXED, __HIP_MEMORY_SCOPE_WORKGROUP) == 0u && __builtin_amdgcn_s_memrealtime() - t_entry < T_READ) __builtin_amdgcn_s_sleep(16);
    const unsigned tag = __float_as_uint(lds[L_SC + 3]);
    const unsigned ta = __float_as_uint(lds[L_PC + 0]), tb = __float_as_uint(lds[L_PC + 1]);
    if (lane == 0 && tb != 0u)
      __hip_atomic_store((GAS unsigned long long*)(p.ws + WS_PACE + b * 256 + h * 8), ((unsigned long long)tag << 32) | (tb << 16) | ta, __ATOMIC_RELAXED, __HIP_MEMORY_SCOPE_AGENT);
    while (__builtin_amdgcn_s_memrealtime() - t_entry < T_READ) __builtin_amdgcn_s_sleep(16);
    typedef unsigned u32x16 __attribute__((ext_vector_type(16)));
    const __attribute__((address_space(4))) u32x16* sp = (const __attribute__((address_space(4))) u32x16*)(p.ws + WS_PACE + b * 256);
    unsigned sa = 0, sb = 0, n = 0;
#pragma unroll
    for (int q = 0; q < 4; ++q) {
      const u32x16 v = sp[q];
#pragma unroll
      for (int k = 0; k < 8; ++k) {
        const bool ok = v[2 * k + 1] == tag;
        sa += ok ? (v[2 * k] & 0xffffu) : 0u; sb += ok ? (v[2 * k] >> 16) : 0u; n += ok ? 1u : 0u;
      }
    }
    if (n >= 16u) {
      sa /= n; sb /= n;
      const unsigned slope = ((sb - sa) * ADAPT_Q) / (256u * (PB - PA));
      const unsigned at_pc = sb + ((sb - sa) * (PC - PB)) / (PB - PA);
      const unsigned line_pc = PACE_T0 + PC * PACE_DT, line_end = PACE_T0 + (TILES - 2) * PACE_DT;
      const unsigned end = at_pc + slope * (TILES - 2 - PC);
      if (end > line_end + ADAPT_M) {
        const unsigned base = max(at_pc > ADAPT_M ? at_pc - ADAPT_M : 0u, line_pc);
        const unsigned nslope = (end - ADAPT_M - base + (TILES - 2 - PC) / 2) / (TILES - 2 - PC);
        if (lane == 0) { lds[L_PC + 2] = __uint_as_float(base); lds[L_PC + 3] = __uint_as_float(max(nslope, 100u)); }
      }
    }
  }
  STAMP(2);
  u32x4 WB[8][4];
  {
    const __amdgpu_buffer_rsrc_t ro = rsrc(p.wo, NH * DH * DM * 2);
    const int voff = (128 * h + g) * 1024 + 256 * hw + 16 * c;
    float* st = &lds[L_ST + hw * 2048];
#pragma unroll
    for (int s = 0; s < 4; ++s) {
#pragma unroll
      for (int hf = 0; hf < 2; ++hf) {
#pragma unroll
        for (int i = 0; i < 4; ++i) W[i] = __builtin_amdgcn_raw_buffer_load_b128(ro, voff, (32 * s + 16 * hf + 4 * i) * 1024, AUX_NT);
#pragma unroll
        for (int i = 0; i < 4; ++i) *reinterpret_cast<u32x4*>(&st[(16 * hf + 4 * i + g) * 64 + 4 * c]) = W[i];
      }
      asm volatile("s_waitcnt lgkmcnt(0)" ::: "memory");
#pragma unroll
      for (int t = 0; t < 8; ++t) {
        const float* a0 = &st[(8 * g + (c >> 2)) * 64 + 8 * t + 2 * (c & 3)];
        const s16x4 u0 = __builtin_amdgcn_ds_read_tr16_b64_v4i16((LAS s16x4*)a0);
        const s16x4 u1 = __builtin_amdgcn_ds_read_tr16_b64_v4i16((LAS s16x4*)(a0 + 4 * 64));
        const u32x2 x0 = __builtin_bit_cast(u32x2, u0), x1 = __builtin_bit_cast(u32x2, u1);
        WB[t][s] = u32x4{x0[0], x0[1], x1[0], x1[1]};
      }
      asm volatile("s_waitcnt lgkmcnt(0)" ::: "memory");
    }
  }
  STAMP(3);
  wg_barrier();
  STAMP(4);
  {
    float mw[NSW], lw[NSW];
#pragma unroll
    for (int w = 0; w < NSW; ++w) { mw[w] = lds[L_MRG + w * 132 + 128]; lw[w] = lds[L_MRG + w * 132 + 129]; }
    float M = mw[0];
#pragma unroll
    for (int w = 1; w < NSW; ++w) M = fmaxf(M, mw[w]);
    float L = 0.f, o0 = 0.f, o1 = 0.f;
#pragma unroll
    for (int w = 0; w < NSW; ++w) {
      const float wt = __builtin_amdgcn_exp2f(mw[w] - M);
      const u32x2 ov = *reinterpret_cast<const u32x2*>(&lds[L_MRG + w * 132 + 32 * g + 2 * c]);
      L = fmaf(wt, lw[w], L); o0 = fmaf(wt, __uint_as_float(ov[0]), o0); o1 = fmaf(wt, __uint_as_float(ov[1]), o1);
    }
    const float inv = __builtin_amdgcn_rcpf(L);
    float* ob = &lds[L_H + hw * 64];
    ob[16 * g + c] = __uint_as_float(pk_bf16(o0 * inv, o1 * inv));
    asm volatile("s_waitcnt lgkmcnt(0)" ::: "memory");
    u32x4 oa[4];
#pragma unroll
    for (int s = 0; s < 4; ++s) oa[s] = *reinterpret_cast<const u32x4*>(&ob[16 * s + 4 * g]);
    f32x4 d[8];
#pragma unroll
    for (int t = 0; t < 8; ++t) d[t] = f32x4{0, 0, 0, 0};
#pragma unroll
    for (int s = 0; s < 4; ++s)
#pragma unroll
      for (int t = 0; t < 8; ++t) d[t] = mfma(oa[s], WB[t][s], d[t]);
    unsigned m1 = (g & 1) ? ~0u : 0u, m2 = (g & 2) ? ~0u : 0u;
    asm volatile("" : "+v"(m1), "+v"(m2));
    const float gate = lds[L_SC + 0], yc = lds[L_SC + 1] * lds[L_SC + 2];
    const unsigned tag = __float_as_uint(lds[L_SC + 3]);
    const __amdgpu_buffer_rsrc_t rin = rsrc(p.ws + WS_INBOX + (size_t)b * 32 * 4096, 32 * 4096);
#pragma unroll
    for (int pi = 0; pi < 2; ++pi) {
      const unsigned lo = (m1 & __float_as_uint(d[2 + pi][0])) | (~m1 & __float_as_uint(d[pi][0]));
      const unsigned hi = (m1 & __float_as_uint(d[6 + pi][0])) | (~m1 & __float_as_uint(d[4 + pi][0]));
      const float a0 = __uint_as_float((m2 & hi) | (~m2 & lo));
      const float a1 = dppf<ROR8>(a0);
      if (c < 8) {
        const float v0 = gate * a0 + yc * dy[2 * pi], v1 = gate * a1 + yc * dy[2 * pi + 1];
        const u32x4 gr = {__float_as_uint(v0), tag, __float_as_uint(v1), tag};
        __builtin_amdgcn_raw_buffer_store_b128(gr, rin, (8 * hw + 2 * g + pi) * 4096 + h * 128 + 16 * c, 0, AUX_SC1);
      }
    }
    STAMP(5);
    STAMP_FLUSH();
  }
}

extern "C" void kernel_launch(void* const* d_in, const int* in_sizes, int n_in, void* d_out, int out_size, void* d_ws, size_t ws_size,
                              hipStream_t stream) {
  Params p;
  p.xA = (const unsigned short*)d_in[0]; p.K = (const unsigned short*)d_in[1]; p.V = (const unsigned short*)d_in[2];
  p.wq = (const unsigned short*)d_in[3]; p.wo = (const unsigned short*)d_in[4];
  p.wr = (const unsigned short*)d_in[6]; p.wg = (const unsigned short*)d_in[7]; p.wu = (const unsigned short*)d_in[8];
  p.wd = (const unsigned short*)d_in[9]; p.gw = (const unsigned short*)d_in[10];
  p.out = (unsigned short*)d_out; p.ws = (unsigned char*)d_ws;
  fused_decode_kernel<<<NB * NH, NT, 0, stream>>>(p);
}
```

```cpp
#include <hip/hip_runtime.h>
#include <hip/hip_bf16.h>

typedef unsigned u32x4 __attribute__((ext_vector_type(4)));
typedef unsigned u32x2 __attribute__((ext_vector_type(2)));
typedef float f32x4 __attribute__((ext_vector_type(4)));
typedef short s16x4 __attribute__((ext_vector_type(4)));
typedef __bf16 bf16x2 __attribute__((ext_vector_type(2)));
typedef __bf16 bf16x8 __attribute__((ext_vector_type(8)));
#define GAS __attribute__((address_space(1)))
#define LAS __attribute__((address_space(3)))
typedef GAS unsigned gu32;

constexpr int NB = 8, NH = 32, SK = 4096, DH = 128, DM = 512, FF = 1792;
constexpr int NSW = 4;
constexpr int NT = (NSW + 4) * 64;
constexpr int TILES = SK / 16 / NSW;
constexpr int BURST = 3;
constexpr int HD = 8;
constexpr int HD2 = 4;
#define ADAPT 1
#define ADAPT_M 100
#define ADAPT_Q 248
constexpr int PA = 8, PB = 22, PC = 30, T_READ = 3300;
#define CODE_PF 15360
#define CODE_PF_NT 0
#define CODE_PF_WAVE 3
#define PACE_DT 117
#define PACE_T0 160
constexpr float C2 = 0.08838834764831845f * 1.4426950408889634f;
constexpr int RSRC3 = 0x00020000;
constexpr int AUX_NT = 2, AUX_SC1 = 16;
constexpr int WS_EPOCH = 0, WS_INBOX = 4096, WS_PACE = WS_INBOX + (1 << 20);
constexpr unsigned SPIN_CAP = 1u << 16;

struct Params {
  const unsigned short *K, *V, *xA, *wq, *wo, *wr, *wg, *wu, *wd, *gw;
  unsigned short* out;
  unsigned char* ws;
};

constexpr int L_QP = 0;
constexpr int L_MRG = L_QP + 512;
constexpr int L_GU = L_MRG + NSW * 132;
constexpr int L_H = L_GU + 512;
constexpr int L_SC = L_H + 256;
constexpr int L_PC = L_SC + 8;
constexpr int L_ST = L_PC + 4;
constexpr int L_END = L_ST + 4 * 2048;

__device__ __forceinline__ float dot2(unsigned a, unsigned b, float c) {
  return __builtin_amdgcn_fdot2_f32_bf16(__builtin_bit_cast(bf16x2, a), __builtin_bit_cast(bf16x2, b), c, false);
}
__device__ __forceinline__ unsigned pk_bf16(float lo, float hi) {
  bf16x2 v = {(__bf16)lo, (__bf16)hi};
  return __builtin_bit_cast(unsigned, v);
}
__device__ __forceinline__ float rnd_bf16(float x) { return (float)(__bf16)x; }
__device__ __forceinline__ float bf_lo(unsigned u) { return __builtin_bit_cast(float, u << 16); }
__device__ __forceinline__ float bf_hi(unsigned u) { return __builtin_bit_cast(float, u & 0xffff0000u); }
template <int CTRL> __device__ __forceinline__ float dppf(float x) {
  return __builtin_bit_cast(float, __builtin_amdgcn_mov_dpp(__builtin_bit_cast(int, x), CTRL, 0xf, 0xf, true));
}
template <int CTRL> __device__ __forceinline__ unsigned dppu(unsigned x) {
  return (unsigned)__builtin_amdgcn_mov_dpp((int)x, CTRL, 0xf, 0xf, true);
}
constexpr int XOR1 = 0xB1, XOR2 = 0x4E, XOR7 = 0x141, ROR8 = 0x128, ROR4 = 0x124, BCAST = 0x150;
__device__ __forceinline__ float xrow_sum(float x) {
  auto s = __builtin_amdgcn_permlane16_swap(__float_as_uint(x), __float_as_uint(x), false, false);
  x = __uint_as_float(s[0]) + __uint_as_float(s[1]);
  auto t = __builtin_amdgcn_permlane32_swap(__float_as_uint(x), __float_as_uint(x), false, false);
  return __uint_as_float(t[0]) + __uint_as_float(t[1]);
}
__device__ __forceinline__ float xrow_max(float x) {
  auto s = __builtin_amdgcn_permlane16_swap(__float_as_uint(x), __float_as_uint(x), false, false);
  x = fmaxf(__uint_as_float(s[0]), __uint_as_float(s[1]));
  auto t = __builtin_amdgcn_permlane32_swap(__float_as_uint(x), __float_as_uint(x), false, false);
  return fmaxf(__uint_as_float(t[0]), __uint_as_float(t[1]));
}
__device__ __forceinline__ float wave_sum(float x) {
  x += dppf<XOR1>(x); x += dppf<XOR2>(x); x += dppf<XOR7>(x); x += dppf<ROR8>(x);
  return xrow_sum(x);
}
__device__ __forceinline__ f32x4 mfma(u32x4 a, u32x4 b, f32x4 c) {
  return __builtin_amdgcn_mfma_f32_16x16x32_bf16(__builtin_bit_cast(bf16x8, a), __builtin_bit_cast(bf16x8, b), c, 0, 0, 0);
}
__device__ __forceinline__ void wg_barrier() {
  asm volatile("s_waitcnt lgkmcnt(0)" ::: "memory");
  __builtin_amdgcn_s_barrier();
  asm volatile("" ::: "memory");
}
__device__ __forceinline__ float sigmoidf_(float x) { return 1.f / (1.f + __expf(-x)); }
__device__ __forceinline__ float safe_max(float m) { return m == -INFINITY ? 0.f : m; }
__device__ __forceinline__ __amdgpu_buffer_rsrc_t rsrc(const void* p, int bytes) {
  return __builtin_amdgcn_make_buffer_rsrc(const_cast<void*>(p), 0, bytes, RSRC3);
}
__device__ __forceinline__ f32x4 vm_step(u32x4 w, unsigned pp, const u32x4& msk, f32x4 d) {
  u32x4 bq = {pp & msk[0], pp & msk[1], pp & msk[2], pp & msk[3]};
  return mfma(w, bq, d);
}
__device__ __forceinline__ unsigned rep16(unsigned pair, int odd) {
  return odd ? __builtin_amdgcn_perm(pair, pair, 0x03020302u) : __builtin_amdgcn_perm(pair, pair, 0x01000100u);
}
__device__ __forceinline__ u32x4 ldw(const unsigned short* p) { return __builtin_nontemporal_load(reinterpret_cast<const u32x4*>(p)); }
#define PIN() __builtin_amdgcn_sched_barrier(0)

#define STAMP_FLUSH() do {} while (0)
#define STAMP(i) do {} while (0)

__global__ void __launch_bounds__(NT, NT / 256) fused_decode_kernel(Params p) {
  __shared__ __attribute__((aligned(16))) float lds[L_END];
  const int tid = threadIdx.x, lane = tid & 63, wave = __builtin_amdgcn_readfirstlane(tid >> 6);
  const int g = lane >> 4, c = lane & 15;
  const int bid = blockIdx.x, h = (bid & 7) * 4 + ((bid >> 3) & 3), b = bid >> 5;
  STAMP(0);
  const unsigned long long t_entry = __builtin_amdgcn_s_memrealtime();
  unsigned long long pc0;
  asm volatile("s_getpc_b64 %0" : "=s"(pc0));
  const unsigned short *Kp = p.K, *Vp = p.V;
  asm volatile("" : "+s"(Kp), "+s"(Vp));

  if (__builtin_expect(wave < NSW, 1)) {
    const size_t bh = (size_t)(b * NH + h) * SK * DH;
    const __amdgpu_buffer_rsrc_t rk = rsrc(Kp + bh, SK * DH * 2), rv = rsrc(Vp + bh, SK * DH * 2);
    const int voff = lane * 16;
    unsigned cpf[2] = {0u, 0u};
    static_assert(CODE_PF <= 16384, "two loads; CODE_PF must not exceed the length of the kernel's code");
    if (wave == CODE_PF_WAVE) {
      const unsigned char* pc = (const unsigned char*)(pc0 & ~127ull);
#pragma unroll
      for (int i = 0; i < (CODE_PF + 8191) / 8192; ++i)
        if (i * 8192 + lane * 128 < CODE_PF) {
          cpf[i] = *(const GAS unsigned*)(pc + i * 8192 + lane * 128);
        }
    }
    auto ldk = [&](int i, int j) { return __builtin_amdgcn_raw_buffer_load_b128(rk, voff, (i * NSW + wave) * 4096 + j * 1024, AUX_NT); };
    auto ldv = [&](int i, int j) { return __builtin_amdgcn_raw_buffer_load_b128(rv, voff, (i * NSW + wave) * 4096 + j * 1024, AUX_NT); };
    u32x4 RK[4], RV[4], EK[BURST][4], EV[BURST][4];
#pragma unroll
    for (int j = 0; j < 4; ++j) RK[j] = ldk(BURST, j);
#pragma unroll
    for (int j = 0; j < 4; ++j) RV[j] = ldv(BURST, j);
#pragma unroll
    for (int e = 0; e < BURST; ++e) {
#pragma unroll
      for (int j = 0; j < 4; ++j) EK[e][j] = ldk(e, j);
#pragma unroll
      for (int j = 0; j < 4; ++j) EV[e][j] = ldv(e, j);
    }
    STAMP(7);
    wg_barrier();
    STAMP(1);
    asm volatile("" :: "v"(cpf[0]), "v"(cpf[1]));
    unsigned qp[4];
    {
      f32x4 q0 = {0, 0, 0, 0}, q1 = {0, 0, 0, 0};
#pragma unroll
      for (int w = 0; w < 4; ++w) {
        q0 += *reinterpret_cast<const f32x4*>(&lds[L_QP + w * 128 + 8 * c]);
        q1 += *reinterpret_cast<const f32x4*>(&lds[L_QP + w * 128 + 8 * c + 4]);
      }
      qp[0] = pk_bf16(q0[0], q0[1]); qp[1] = pk_bf16(q0[2], q0[3]); qp[2] = pk_bf16(q1[0], q1[1]); qp[3] = pk_bf16(q1[2], q1[3]);
    }
    float m = -INFINITY, ms = 0.f, l = 0.f, acc[8];
#pragma unroll
    for (int k = 0; k < 8; ++k) acc[k] = 0.f;
    const bool b8 = c & 8, b4 = c & 4;

    auto process = [&](u32x4 (&TK)[4], u32x4 (&TV)[4], bool refill, int inext) {
      float x[4];
#pragma unroll
      for (int j = 0; j < 4; ++j) {
        float d = dot2(TK[j][0], qp[0], 0.f);
        d = dot2(TK[j][1], qp[1], d); d = dot2(TK[j][2], qp[2], d); d = dot2(TK[j][3], qp[3], d);
        x[j] = d;
        if (refill) { TK[j] = ldk(inext, j); PIN(); }
      }
      const float a0 = x[0] + dppf<ROR8>(x[0]), a1 = x[1] + dppf<ROR8>(x[1]), a2 = x[2] + dppf<ROR8>(x[2]), a3 = x[3] + dppf<ROR8>(x[3]);
      const float y0 = b8 ? a2 : a0, y1 = b8 ? a3 : a1;
      const float z0 = y0 + dppf<XOR7>(y0), z1 = y1 + dppf<XOR7>(y1);
      float z = b4 ? z1 : z0;
      z += dppf<XOR2>(z); z += dppf<XOR1>(z);
      const float sc = z * C2;
      float pm = fmaxf(sc, dppf<ROR4>(sc)); pm = fmaxf(pm, dppf<ROR8>(pm));
      if (__any(pm > m)) {
        const float mn = fmaxf(m, pm), msn = safe_max(mn), al = __builtin_amdgcn_exp2f(m == -INFINITY ? -INFINITY : m - msn);
        l *= al;
#pragma unroll
        for (int k = 0; k < 8; ++k) acc[k] *= al;
        m = mn; ms = msn;
      }
      const float pe = __builtin_amdgcn_exp2f(sc - ms);
      l += pe;
      const unsigned plo = pk_bf16(pe, 0.f), phi = plo << 16;
#define PV(j) { const unsigned lo_ = dppu<BCAST + 4 * (j)>(plo), hi_ = dppu<BCAST + 4 * (j)>(phi); \
        _Pragma("unroll") for (int k = 0; k < 4; ++k) { acc[2 * k] = dot2(TV[j][k], lo_, acc[2 * k]); acc[2 * k + 1] = dot2(TV[j][k], hi_, acc[2 * k + 1]); } \
        if (refill) { TV[j] = ldv(inext, j); PIN(); } }
      PV(0) PV(1) PV(2) PV(3)
#undef PV
      asm volatile("" : "+v"(acc[0]), "+v"(acc[1]), "+v"(acc[2]), "+v"(acc[3]), "+v"(acc[4]), "+v"(acc[5]), "+v"(acc[6]), "+v"(acc[7]));
    };
    process(RK, RV, true, BURST + 1);
    STAMP(2);
#pragma unroll
    for (int e = 0; e < BURST; ++e) process(EK[e], EV[e], false, 0);
    unsigned long long tnext = t_entry + PACE_T0 + BURST * PACE_DT;
    unsigned dt = PACE_DT;
    for (int i = BURST + 1; i < TILES - 1; ++i) {
      tnext += dt;
      unsigned long long now = __builtin_amdgcn_s_memrealtime();
      while ((long long)(now - tnext) < 0) { __builtin_amdgcn_s_sleep(1); now = __builtin_amdgcn_s_memrealtime(); }
      if (wave == 0 && (i == PA || i == PB)) { if (lane == 0) lds[L_PC + (i == PB)] = __uint_as_float((unsigned)(now - t_entry)); }
      if (i == PC) {
        const unsigned nb = __builtin_amdgcn_readfirstlane(__float_as_uint(lds[L_PC + 2])), ns = __builtin_amdgcn_readfirstlane(__float_as_uint(lds[L_PC + 3]));
        if (ns != 0) { tnext = t_entry + nb; dt = ns; }
      }
      process(RK, RV, true, i + 1);
    }
    process(RK, RV, false, 0);
    STAMP(3);
    unsigned xfin = 0;
    if (wave == 0 && lane < 16) xfin = p.xA[b * DM + 16 * h + lane];
    {
      const float M = xrow_max(m), sc = __builtin_amdgcn_exp2f(m - safe_max(M));
      float ls = l * sc;
      ls += dppf<ROR4>(ls); ls += dppf<ROR8>(ls);
      ls = xrow_sum(ls);
      float o[8];
#pragma unroll
      for (int k = 0; k < 8; ++k) o[k] = xrow_sum(acc[k] * sc);
      float* rec = &lds[L_MRG + wave * 132];
      if (g == 0) {
        *reinterpret_cast<f32x4*>(rec + 8 * c) = f32x4{o[0], o[1], o[2], o[3]};
        *reinterpret_cast<f32x4*>(rec + 8 * c + 4) = f32x4{o[4], o[5], o[6], o[7]};
        if (c == 0) { rec[128] = M; rec[129] = ls; }
      }
    }
    wg_barrier();
    STAMP(4);
    if (wave != 0) { STAMP_FLUSH(); return; }
    const unsigned tag = __float_as_uint(lds[L_SC + 3]);
    const __amdgpu_buffer_rsrc_t rin = rsrc(p.ws + WS_INBOX + (size_t)(b * 32 + h) * 4096, 4096);
    u32x4 gv[4];
#pragma unroll
    for (int q = 0; q < 4; ++q) gv[q] = u32x4{0u, ~tag, 0u, ~tag};
    for (unsigned spins = 0;; ++spins) {
#pragma unroll
      for (int q = 0; q < 4; ++q)
        if (gv[q][1] != tag || gv[q][3] != tag) gv[q] = __builtin_amdgcn_raw_buffer_load_b128(rin, lane * 16 + q * 1024, 0, AUX_SC1);
      int missing = 0;
#pragma unroll
      for (int q = 0; q < 4; ++q) missing += __popcll(__ballot(gv[q][1] != tag || gv[q][3] != tag));
      if (missing == 0 || spins > SPIN_CAP) break;
      if (missing > 32) __builtin_amdgcn_s_sleep(16);
    }
    STAMP(5);
    float s0 = 0.f, s1 = 0.f;
#pragma unroll
    for (int q = 0; q < 4; ++q) { s0 += __uint_as_float(gv[q][0]); s1 += __uint_as_float(gv[q][2]); }
    s0 += dppf<ROR8>(s0); s1 += dppf<ROR8>(s1);
    s0 = xrow_sum(s0); s1 = xrow_sum(s1);
    if (lane < 16) {
      const float s = lane < 8 ? s0 : s1;
      const float xb = __builtin_bit_cast(float, xfin << 16);
      const float r = s + lds[L_SC + 1] * (xb + xb * 0.f);
      p.out[b * DM + 16 * h + lane] = __builtin_bit_cast(unsigned short, (__bf16)r);
    }
    STAMP(6);
    if (h == 0 && lane == 0) __hip_atomic_store((gu32*)(p.ws + WS_EPOCH + b * 256), tag, __ATOMIC_RELAXED, __HIP_MEMORY_SCOPE_AGENT);
    STAMP_FLUSH();
    return;
  }

  const int hw = wave - NSW;
  __builtin_amdgcn_s_setprio(3);
  u32x4 msk;
#pragma unroll
  for (int w = 0; w < 4; ++w) msk[w] = (c < 8 && (c >> 1) == w) ? ((c & 1) ? 0xffff0000u : 0x0000ffffu) : 0u;
  u32x4 W[16], xp[4];
  {
    const __amdgpu_buffer_rsrc_t rq = rsrc(p.wq, DM * NH * DH * 2);
    const u32x4* xs = reinterpret_cast<const u32x4*>(p.xA + b * DM + 128 * hw + 32 * g);
#pragma unroll
    for (int i = 0; i < 4; ++i) xp[i] = xs[i];
    const int voff = (128 * hw + 32 * g) * 8192 + h * 256 + 16 * c;
#pragma unroll
    for (int i = 0; i < HD; ++i) W[i] = __builtin_amdgcn_raw_buffer_load_b128(rq, voff, i * 8192, AUX_NT);
    if (hw == 0 && lane == 0) lds[L_SC + 4] = 0.f;
    if (hw == 0 && lane < 4) lds[L_PC + lane] = 0.f;
    f32x4 d = {0, 0, 0, 0};
#pragma unroll
    for (int i = 0; i < 32; ++i) {
      d = vm_step(W[i % HD], rep16(xp[i >> 3][(i >> 1) & 3], i & 1), msk, d);
      if (i + HD < 32) W[i % HD] = __builtin_amdgcn_raw_buffer_load_b128(rq, voff, (i + HD) * 8192, AUX_NT);
      PIN();
    }
    if (c < 8) {
#pragma unroll
      for (int r = 0; r < 4; ++r) lds[L_QP + hw * 128 + 32 * g + 8 * r + c] = d[r];
    }
  }
  STAMP(1);
  __builtin_amdgcn_s_setprio(0);
  wg_barrier();
  if (hw == 2 && lane == 0) {
    const unsigned epoch = __hip_atomic_load((gu32*)(p.ws + WS_EPOCH + b * 256), __ATOMIC_RELAXED, __HIP_MEMORY_SCOPE_AGENT);
    lds[L_SC + 3] = __uint_as_float(epoch + 1u);
  }
  if (hw == 3) {
    const u32x4 xa = *reinterpret_cast<const u32x4*>(p.xA + b * DM + 8 * lane), gw = *reinterpret_cast<const u32x4*>(p.gw + 8 * lane);
    const u32x4 wr = *reinterpret_cast<const u32x4*>(p.wr + 8 * lane);
    float sg = 0.f, sr = 0.f;
#pragma unroll
    for (int k = 0; k < 4; ++k) {
      sg += rnd_bf16(bf_lo(xa[k]) * bf_lo(gw[k])) + rnd_bf16(bf_hi(xa[k]) * bf_hi(gw[k]));
      sr = dot2(xa[k], wr[k], sr);
    }
    sg = wave_sum(sg); sr = wave_sum(sr);
    const float gate = rnd_bf16(sigmoidf_(rnd_bf16(sg)));
    const float rs = rnd_bf16(sigmoidf_(rnd_bf16(sr)));
    if (lane == 0) { lds[L_SC + 0] = gate; lds[L_SC + 1] = rnd_bf16(1.f - gate); lds[L_SC + 2] = rs; }
  }
  f32x4 dy = {0, 0, 0, 0};
  if (h < FF / 64) {
    {
      f32x4 d = {0, 0, 0, 0};
      const unsigned short* wbase = (c < 8 ? p.wg : p.wu) + 64 * h + 8 * (c & 7) + (size_t)(128 * hw + 32 * g) * FF;
#pragma unroll
      for (int i = 0; i < HD2; ++i) W[i] = ldw(wbase + (size_t)i * FF);
#pragma unroll
      for (int i = 0; i < 32; ++i) {
        d = vm_step(W[i % HD2], rep16(xp[i >> 3][(i >> 1) & 3], i & 1), msk, d);
        if (i + HD2 < 32) W[i % HD2] = ldw(wbase + (size_t)(i + HD2) * FF);
        PIN();
      }
      if (c < 8) {
#pragma unroll
        for (int r = 0; r < 4; ++r) lds[L_GU + hw * 128 + 32 * g + 8 * r + c] = d[r];
      }
      asm volatile("s_waitcnt lgkmcnt(0)" ::: "memory");
      if (lane == 0) __hip_atomic_fetch_add((LAS unsigned*)&lds[L_SC + 4], 1u, __ATOMIC_RELAXED, __HIP_MEMORY_SCOPE_WORKGROUP);
    }
    const __amdgpu_buffer_rsrc_t rd_ = rsrc(p.wd, FF * DM * 2);
    const int voff = (64 * h + 16 * g) * 1024 + 256 * hw + 16 * c;
#pragma unroll
    for (int i = 0; i < 16; ++i) {
      W[i] = __builtin_amdgcn_raw_buffer_load_b128(rd_, voff, i * 1024, AUX_NT);
      if ((i % HD2) == HD2 - 1) asm volatile("s_waitcnt vmcnt(0)" ::: "memory");
    }
    while (__hip_atomic_load((LAS unsigned*)&lds[L_SC + 4], __ATOMIC_RELAXED, __HIP_MEMORY_SCOPE_WORKGROUP) < 4u) __builtin_amdgcn_s_sleep(4);
    asm volatile("" ::: "memory");
    float gt = 0.f, up = 0.f;
#pragma unroll
    for (int w = 0; w < 4; ++w) { gt += lds[L_GU + w * 128 + lane]; up += lds[L_GU + w * 128 + 64 + lane]; }
    gt = rnd_bf16(gt); up = rnd_bf16(up);
    const float si = rnd_bf16(gt * sigmoidf_(gt));
    lds[L_H + hw * 64 + lane] = __uint_as_float(pk_bf16(si * up, si * up));
    asm volatile("s_waitcnt lgkmcnt(0)" ::: "memory");
    u32x4 hp[4];
#pragma unroll
    for (int i = 0; i < 4; ++i) hp[i] = *reinterpret_cast<const u32x4*>(&lds[L_H + hw * 64 + 16 * g + 4 * i]);
#pragma unroll
    for (int i = 0; i < 16; ++i) dy = vm_step(W[i], hp[i >> 2][i & 3], msk, dy);
  }
  if (hw == 2) {
    while (__hip_atomic_load((LAS unsigned*)&lds[L_PC + 1], __ATOMIC_RELAXED, __HIP_MEMORY_SCOPE_WORKGROUP) == 0u && __builtin_amdgcn_s_memrealtime() - t_entry < T_READ) __builtin_amdgcn_s_sleep(16);
    const unsigned tag = __float_as_uint(lds[L_SC + 3]);
    const unsigned ta = __float_as_uint(lds[L_PC + 0]), tb = __float_as_uint(lds[L_PC + 1]);
    if (lane == 0 && tb != 0u)
      __hip_atomic_store((GAS unsigned long long*)(p.ws + WS_PACE + b * 256 + h * 8), ((unsigned long long)tag << 32) | (tb << 16) | ta, __ATOMIC_RELAXED, __HIP_MEMORY_SCOPE_AGENT);
    while (__builtin_amdgcn_s_memrealtime() - t_entry < T_READ) __builtin_amdgcn_s_sleep(16);
    typedef unsigned u32x16 __attribute__((ext_vector_type(16)));
    const __attribute__((address_space(4))) u32x16* sp = (const __attribute__((address_space(4))) u32x16*)(p.ws + WS_PACE + b * 256);
    unsigned sa = 0, sb = 0, n = 0;
#pragma unroll 1
    for (int q = 0; q < 4; ++q) {
      const u32x16 v = sp[q];
#pragma unroll
      for (int k = 0; k < 8; ++k) {
        const bool ok = v[2 * k + 1] == tag;
        sa += ok ? (v[2 * k] & 0xffffu) : 0u; sb += ok ? (v[2 * k] >> 16) : 0u; n += ok ? 1u : 0u;
      }
    }
    if (n >= 16u) {
      sa /= n; sb /= n;
      const unsigned slope = ((sb - sa) * ADAPT_Q) / (256u * (PB - PA));
      const unsigned at_pc = sb + ((sb - sa) * (PC - PB)) / (PB - PA);
      const unsigned line_pc = PACE_T0 + PC * PACE_DT, line_end = PACE_T0 + (TILES - 2) * PACE_DT;
      const unsigned end = at_pc + slope * (TILES - 2 - PC);
      if (end > line_end + ADAPT_M) {
        const unsigned base = min(max(at_pc - ADAPT_M, line_pc), line_pc + 2000u);
        const unsigned nslope = (end - ADAPT_M - min(base, end - ADAPT_M) + (TILES - 2 - PC) / 2) / (TILES - 2 - PC);
        if (lane == 0) { lds[L_PC + 2] = __uint_as_float(base); lds[L_PC + 3] = __uint_as_float(min(max(nslope, 100u), 200u)); }
      }
    }
  }
  STAMP(2);
  u32x4 WB[8][4];
  {
    const __amdgpu_buffer_rsrc_t ro = rsrc(p.wo, NH * DH * DM * 2);
    const int voff = (128 * h + g) * 1024 + 256 * hw + 16 * c;
    float* st = &lds[L_ST + hw * 2048];
#pragma unroll
    for (int s = 0; s < 4; ++s) {
#pragma unroll
      for (int hf = 0; hf < 2; ++hf) {
#pragma unroll
        for (int i = 0; i < 4; ++i) W[i] = __builtin_amdgcn_raw_buffer_load_b128(ro, voff, (32 * s + 16 * hf + 4 * i) * 1024, AUX_NT);
#pragma unroll
        for (int i = 0; i < 4; ++i) *reinterpret_cast<u32x4*>(&st[(16 * hf + 4 * i + g) * 64 + 4 * c]) = W[i];
      }
      asm volatile("s_waitcnt lgkmcnt(0)" ::: "memory");
#pragma unroll
      for (int t = 0; t < 8; ++t) {
        const float* a0 = &st[(8 * g + (c >> 2)) * 64 + 8 * t + 2 * (c & 3)];
        const s16x4 u0 = __builtin_amdgcn_ds_read_tr16_b64_v4i16((LAS s16x4*)a0);
        const s16x4 u1 = __builtin_amdgcn_ds_read_tr16_b64_v4i16((LAS s16x4*)(a0 + 4 * 64));
        const u32x2 x0 = __builtin_bit_cast(u32x2, u0), x1 = __builtin_bit_cast(u32x2, u1);
        WB[t][s] = u32x4{x0[0], x0[1], x1[0], x1[1]};
      }
      asm volatile("s_waitcnt lgkmcnt(0)" ::: "memory");
    }
  }
  STAMP(3);
  wg_barrier();
  STAMP(4);
  {
    float mw[NSW], lw[NSW];
#pragma unroll
    for (int w = 0; w < NSW; ++w) { mw[w] = lds[L_MRG + w * 132 + 128]; lw[w] = lds[L_MRG + w * 132 + 129]; }
    float M = mw[0];
#pragma unroll
    for (int w = 1; w < NSW; ++w) M = fmaxf(M, mw[w]);
    float L = 0.f, o0 = 0.f, o1 = 0.f;
#pragma unroll
    for (int w = 0; w < NSW; ++w) {
      const float wt = __builtin_amdgcn_exp2f(mw[w] - M);
      const u32x2 ov = *reinterpret_cast<const u32x2*>(&lds[L_MRG + w * 132 + 32 * g + 2 * c]);
      L = fmaf(wt, lw[w], L); o0 = fmaf(wt, __uint_as_float(ov[0]), o0); o1 = fmaf(wt, __uint_as_float(ov[1]), o1);
    }
    const float inv = __builtin_amdgcn_rcpf(L);
    float* ob = &lds[L_H + hw * 64];
    ob[16 * g + c] = __uint_as_float(pk_bf16(o0 * inv, o1 * inv));
    asm volatile("s_waitcnt lgkmcnt(0)" ::: "memory");
    u32x4 oa[4];
#pragma unroll
    for (int s = 0; s < 4; ++s) oa[s] = *reinterpret_cast<const u32x4*>(&ob[16 * s + 4 * g]);
    f32x4 d[8];
#pragma unroll
    for (int t = 0; t < 8; ++t) d[t] = f32x4{0, 0, 0, 0};
#pragma unroll
    for (int s = 0; s < 4; ++s)
#pragma unroll
      for (int t = 0; t < 8; ++t) d[t] = mfma(oa[s], WB[t][s], d[t]);
    unsigned m1 = (g & 1) ? ~0u : 0u, m2 = (g & 2) ? ~0u : 0u;
    asm volatile("" : "+v"(m1), "+v"(m2));
    const float gate = lds[L_SC + 0], yc = lds[L_SC + 1] * lds[L_SC + 2];
    const unsigned tag = __float_as_uint(lds[L_SC + 3]);
    const __amdgpu_buffer_rsrc_t rin = rsrc(p.ws + WS_INBOX + (size_t)b * 32 * 4096, 32 * 4096);
#pragma unroll
    for (int pi = 0; pi < 2; ++pi) {
      const unsigned lo = (m1 & __float_as_uint(d[2 + pi][0])) | (~m1 & __float_as_uint(d[pi][0]));
      const unsigned hi = (m1 & __float_as_uint(d[6 + pi][0])) | (~m1 & __float_as_uint(d[4 + pi][0]));
      const float a0 = __uint_as_float((m2 & hi) | (~m2 & lo));
      const float a1 = dppf<ROR8>(a0);
      if (c < 8) {
        const float v0 = gate * a0 + yc * dy[2 * pi], v1 = gate * a1 + yc * dy[2 * pi + 1];
        const u32x4 gr = {__float_as_uint(v0), tag, __float_as_uint(v1), tag};
        __builtin_amdgcn_raw_buffer_store_b128(gr, rin, (8 * hw + 2 * g + pi) * 4096 + h * 128 + 16 * c, 0, AUX_SC1);
      }
    }
    STAMP(5);
    STAMP_FLUSH();
  }
}

extern "C" void kernel_launch(void* const* d_in, const int* in_sizes, int n_in, void* d_out, int out_size, void* d_ws, size_t ws_size,
                              hipStream_t stream) {
  Params p;
  p.xA = (const unsigned short*)d_in[0]; p.K = (const unsigned short*)d_in[1]; p.V = (const unsigned short*)d_in[2];
  p.wq = (const unsigned short*)d_in[3]; p.wo = (const unsigned short*)d_in[4];
  p.wr = (const unsigned short*)d_in[6]; p.wg = (const unsigned short*)d_in[7]; p.wu = (const unsigned short*)d_in[8];
  p.wd = (const unsigned short*)d_in[9]; p.gw = (const unsigned short*)d_in[10];
  p.out = (unsigned short*)d_out; p.ws = (unsigned char*)d_ws;
  fused_decode_kernel<<<NB * NH, NT, 0, stream>>>(p);
}
```

```cpp
#include <hip/hip_runtime.h>
#include <hip/hip_bf16.h>

typedef unsigned u32x4 __attribute__((ext_vector_type(4)));
typedef unsigned u32x2 __attribute__((ext_vector_type(2)));
typedef float f32x4 __attribute__((ext_vector_type(4)));
typedef short s16x4 __attribute__((ext_vector_type(4)));
typedef __bf16 bf16x2 __attribute__((ext_vector_type(2)));
typedef __bf16 bf16x8 __attribute__((ext_vector_type(8)));
#define GAS __attribute__((address_space(1)))
#define LAS __attribute__((address_space(3)))
typedef GAS unsigned gu32;

constexpr int NB = 8, NH = 32, SK = 4096, DH = 128, DM = 512, FF = 1792;
constexpr int NSW = 4;
constexpr int NT = (NSW + 4) * 64;
constexpr int TILES = SK / 16 / NSW;
constexpr int BURST = 3;
constexpr int HD = 8;
constexpr int HD2 = 4;
#define FMOVE 1
#define ADAPT 1
#define ADAPT_M 100
#define ADAPT_Q 248
constexpr int PA = 8, PB = 22, PC = 30, T_READ = 3300;
#define CODE_PF 15360
#define CODE_PF_NT 0
#define CODE_PF_WAVE 3
#define PACE_DT 117
#define PACE_T0 160
constexpr float C2 = 0.08838834764831845f * 1.4426950408889634f;
constexpr int RSRC3 = 0x00020000;
constexpr int AUX_NT = 2, AUX_SC1 = 16;
constexpr int WS_EPOCH = 0, WS_INBOX = 4096, WS_PACE = WS_INBOX + (1 << 20);
constexpr unsigned SPIN_CAP = 1u << 16;

struct Params {
  const unsigned short *K, *V, *xA, *wq, *wo, *wr, *wg, *wu, *wd, *gw;
  unsigned short* out;
  unsigned char* ws;
};

constexpr int L_QP = 0;
constexpr int L_MRG = L_QP + 512;
constexpr int L_GU = L_MRG + NSW * 132;
constexpr int L_H = L_GU + 512;
constexpr int L_SC = L_H + 256;
constexpr int L_PC = L_SC + 8;
constexpr int L_ST = L_PC + 4;
constexpr int L_END = L_ST + 4 * 2048;

__device__ __forceinline__ float dot2(unsigned a, unsigned b, float c) {
  return __builtin_amdgcn_fdot2_f32_bf16(__builtin_bit_cast(bf16x2, a), __builtin_bit_cast(bf16x2, b), c, false);
}
__device__ __forceinline__ unsigned pk_bf16(float lo, float hi) {
  bf16x2 v = {(__bf16)lo, (__bf16)hi};
  return __builtin_bit_cast(unsigned, v);
}
__device__ __forceinline__ float rnd_bf16(float x) { return (float)(__bf16)x; }
__device__ __forceinline__ float bf_lo(unsigned u) { return __builtin_bit_cast(float, u << 16); }
__device__ __forceinline__ float bf_hi(unsigned u) { return __builtin_bit_cast(float, u & 0xffff0000u); }
template <int CTRL> __device__ __forceinline__ float dppf(float x) {
  return __builtin_bit_cast(float, __builtin_amdgcn_mov_dpp(__builtin_bit_cast(int, x), CTRL, 0xf, 0xf, true));
}
template <int CTRL> __device__ __forceinline__ unsigned dppu(unsigned x) {
  return (unsigned)__builtin_amdgcn_mov_dpp((int)x, CTRL, 0xf, 0xf, true);
}
constexpr int XOR1 = 0xB1, XOR2 = 0x4E, XOR7 = 0x141, ROR8 = 0x128, ROR4 = 0x124, BCAST = 0x150;
__device__ __forceinline__ float xrow_sum(float x) {
  auto s = __builtin_amdgcn_permlane16_swap(__float_as_uint(x), __float_as_uint(x), false, false);
  x = __uint_as_float(s[0]) + __uint_as_float(s[1]);
  auto t = __builtin_amdgcn_permlane32_swap(__float_as_uint(x), __float_as_uint(x), false, false);
  return __uint_as_float(t[0]) + __uint_as_float(t[1]);
}
__device__ __forceinline__ float xrow_max(float x) {
  auto s = __builtin_amdgcn_permlane16_swap(__float_as_uint(x), __float_as_uint(x), false, false);
  x = fmaxf(__uint_as_float(s[0]), __uint_as_float(s[1]));
  auto t = __builtin_amdgcn_permlane32_swap(__float_as_uint(x), __float_as_uint(x), false, false);
  return fmaxf(__uint_as_float(t[0]), __uint_as_float(t[1]));
}
__device__ __forceinline__ float wave_sum(float x) {
  x += dppf<XOR1>(x); x += dppf<XOR2>(x); x += dppf<XOR7>(x); x += dppf<ROR8>(x);
  return xrow_sum(x);
}
__device__ __forceinline__ f32x4 mfma(u32x4 a, u32x4 b, f32x4 c) {
  return __builtin_amdgcn_mfma_f32_16x16x32_bf16(__builtin_bit_cast(bf16x8, a), __builtin_bit_cast(bf16x8, b), c, 0, 0, 0);
}
__device__ __forceinline__ void wg_barrier() {
  asm volatile("s_waitcnt lgkmcnt(0)" ::: "memory");
  __builtin_amdgcn_s_barrier();
  asm volatile("" ::: "memory");
}
__device__ __forceinline__ float sigmoidf_(float x) { return 1.f / (1.f + __expf(-x)); }
__device__ __forceinline__ float safe_max(float m) { return m == -INFINITY ? 0.f : m; }
__device__ __forceinline__ __amdgpu_buffer_rsrc_t rsrc(const void* p, int bytes) {
  return __builtin_amdgcn_make_buffer_rsrc(const_cast<void*>(p), 0, bytes, RSRC3);
}
__device__ __forceinline__ f32x4 vm_step(u32x4 w, unsigned pp, const u32x4& msk, f32x4 d) {
  u32x4 bq = {pp & msk[0], pp & msk[1], pp & msk[2], pp & msk[3]};
  return mfma(w, bq, d);
}
__device__ __forceinline__ unsigned rep16(unsigned pair, int odd) {
  return odd ? __builtin_amdgcn_perm(pair, pair, 0x03020302u) : __builtin_amdgcn_perm(pair, pair, 0x01000100u);
}
__device__ __forceinline__ u32x4 ldw(const unsigned short* p) { return __builtin_nontemporal_load(reinterpret_cast<const u32x4*>(p)); }
#define PIN() __builtin_amdgcn_sched_barrier(0)

#define STAMP_FLUSH() do {} while (0)
#define STAMP(i) do {} while (0)

__global__ void __launch_bounds__(NT, NT / 256) fused_decode_kernel(Params p) {
  __shared__ __attribute__((aligned(16))) float lds[L_END];
  const int tid = threadIdx.x, lane = tid & 63, wave = __builtin_amdgcn_readfirstlane(tid >> 6);
  const int g = lane >> 4, c = lane & 15;
  const int bid = blockIdx.x, h = (bid & 7) * 4 + ((bid >> 3) & 3), b = bid >> 5;
  STAMP(0);
  const unsigned long long t_entry = __builtin_amdgcn_s_memrealtime();
  unsigned long long pc0;
  asm volatile("s_getpc_b64 %0" : "=s"(pc0));
  const unsigned short *Kp = p.K, *Vp = p.V;
  asm volatile("" : "+s"(Kp), "+s"(Vp));

  if (__builtin_expect(wave < NSW, 1)) {
    const size_t bh = (size_t)(b * NH + h) * SK * DH;
    const __amdgpu_buffer_rsrc_t rk = rsrc(Kp + bh, SK * DH * 2), rv = rsrc(Vp + bh, SK * DH * 2);
    const int voff = lane * 16;
    unsigned cpf[2] = {0u, 0u};
    static_assert(CODE_PF <= 16384, "two loads; CODE_PF must not exceed the length of the kernel's code");
    if (wave == CODE_PF_WAVE) {
      const unsigned char* pc = (const unsigned char*)(pc0 & ~127ull);
#pragma unroll
      for (int i = 0; i < (CODE_PF + 8191) / 8192; ++i)
        if (i * 8192 + lane * 128 < CODE_PF) {
          cpf[i] = *(const GAS unsigned*)(pc + i * 8192 + lane * 128);
        }
    }
    auto ldk = [&](int i, int j) { return __builtin_amdgcn_raw_buffer_load_b128(rk, voff, (i * NSW + wave) * 4096 + j * 1024, AUX_NT); };
    auto ldv = [&](int i, int j) { return __builtin_amdgcn_raw_buffer_load_b128(rv, voff, (i * NSW + wave) * 4096 + j * 1024, AUX_NT); };
    u32x4 RK[4], RV[4], EK[BURST][4], EV[BURST][4];
#pragma unroll
    for (int j = 0; j < 4; ++j) RK[j] = ldk(BURST, j);
#pragma unroll
    for (int j = 0; j < 4; ++j) RV[j] = ldv(BURST, j);
#pragma unroll
    for (int e = 0; e < BURST; ++e) {
#pragma unroll
      for (int j = 0; j < 4; ++j) EK[e][j] = ldk(e, j);
#pragma unroll
      for (int j = 0; j < 4; ++j) EV[e][j] = ldv(e, j);
    }
    STAMP(7);
    wg_barrier();
    STAMP(1);
    asm volatile("" :: "v"(cpf[0]), "v"(cpf[1]));
    unsigned qp[4];
    {
      f32x4 q0 = {0, 0, 0, 0}, q1 = {0, 0, 0, 0};
#pragma unroll
      for (int w = 0; w < 4; ++w) {
        q0 += *reinterpret_cast<const f32x4*>(&lds[L_QP + w * 128 + 8 * c]);
        q1 += *reinterpret_cast<const f32x4*>(&lds[L_QP + w * 128 + 8 * c + 4]);
      }
      qp[0] = pk_bf16(q0[0], q0[1]); qp[1] = pk_bf16(q0[2], q0[3]); qp[2] = pk_bf16(q1[0], q1[1]); qp[3] = pk_bf16(q1[2], q1[3]);
    }
    float m = -INFINITY, ms = 0.f, l = 0.f, acc[8];
#pragma unroll
    for (int k = 0; k < 8; ++k) acc[k] = 0.f;
    const bool b8 = c & 8, b4 = c & 4;

    auto process = [&](u32x4 (&TK)[4], u32x4 (&TV)[4], bool refill, int inext) {
      float x[4];
#pragma unroll
      for (int j = 0; j < 4; ++j) {
        float d = dot2(TK[j][0], qp[0], 0.f);
        d = dot2(TK[j][1], qp[1], d); d = dot2(TK[j][2], qp[2], d); d = dot2(TK[j][3], qp[3], d);
        x[j] = d;
        if (refill) { TK[j] = ldk(inext, j); PIN(); }
      }
      const float a0 = x[0] + dppf<ROR8>(x[0]), a1 = x[1] + dppf<ROR8>(x[1]), a2 = x[2] + dppf<ROR8>(x[2]), a3 = x[3] + dppf<ROR8>(x[3]);
      const float y0 = b8 ? a2 : a0, y1 = b8 ? a3 : a1;
      const float z0 = y0 + dppf<XOR7>(y0), z1 = y1 + dppf<XOR7>(y1);
      float z = b4 ? z1 : z0;
      z += dppf<XOR2>(z); z += dppf<XOR1>(z);
      const float sc = z * C2;
      float pm = fmaxf(sc, dppf<ROR4>(sc)); pm = fmaxf(pm, dppf<ROR8>(pm));
      if (__any(pm > m)) {
        const float mn = fmaxf(m, pm), msn = safe_max(mn), al = __builtin_amdgcn_exp2f(m == -INFINITY ? -INFINITY : m - msn);
        l *= al;
#pragma unroll
        for (int k = 0; k < 8; ++k) acc[k] *= al;
        m = mn; ms = msn;
      }
      const float pe = __builtin_amdgcn_exp2f(sc - ms);
      l += pe;
      const unsigned plo = pk_bf16(pe, 0.f), phi = plo << 16;
#define PV(j) { const unsigned lo_ = dppu<BCAST + 4 * (j)>(plo), hi_ = dppu<BCAST + 4 * (j)>(phi); \
        _Pragma("unroll") for (int k = 0; k < 4; ++k) { acc[2 * k] = dot2(TV[j][k], lo_, acc[2 * k]); acc[2 * k + 1] = dot2(TV[j][k], hi_, acc[2 * k + 1]); } \
        if (refill) { TV[j] = ldv(inext, j); PIN(); } }
      PV(0) PV(1) PV(2) PV(3)
#undef PV
      asm volatile("" : "+v"(acc[0]), "+v"(acc[1]), "+v"(acc[2]), "+v"(acc[3]), "+v"(acc[4]), "+v"(acc[5]), "+v"(acc[6]), "+v"(acc[7]));
    };
    process(RK, RV, true, BURST + 1);
    STAMP(2);
#pragma unroll
    for (int e = 0; e < BURST; ++e) process(EK[e], EV[e], false, 0);
    unsigned long long tnext = t_entry + PACE_T0 + BURST * PACE_DT;
    unsigned dt = PACE_DT;
    for (int i = BURST + 1; i < TILES - 1; ++i) {
      tnext += dt;
      unsigned long long now = __builtin_amdgcn_s_memrealtime();
      while ((long long)(now - tnext) < 0) { __builtin_amdgcn_s_sleep(1); now = __builtin_amdgcn_s_memrealtime(); }
      if (wave == 0 && (i == PA || i == PB)) { if (lane == 0) lds[L_PC + (i == PB)] = __uint_as_float((unsigned)(now - t_entry)); }
      if (i == PC) {
        const unsigned nb = __builtin_amdgcn_readfirstlane(__float_as_uint(lds[L_PC + 2])), ns = __builtin_amdgcn_readfirstlane(__float_as_uint(lds[L_PC + 3]));
        if (ns != 0) { tnext = t_entry + nb; dt = ns; }
      }
      process(RK, RV, true, i + 1);
    }
    process(RK, RV, false, 0);
    STAMP(3);
    unsigned xfin = 0;
    if (wave == 0 && lane < 16) xfin = p.xA[b * DM + 16 * h + lane];
    {
      const float M = xrow_max(m), sc = __builtin_amdgcn_exp2f(m - safe_max(M));
      float ls = l * sc;
      ls += dppf<ROR4>(ls); ls += dppf<ROR8>(ls);
      ls = xrow_sum(ls);
      float o[8];
#pragma unroll
      for (int k = 0; k < 8; ++k) o[k] = xrow_sum(acc[k] * sc);
      float* rec = &lds[L_MRG + wave * 132];
      if (g == 0) {
        *reinterpret_cast<f32x4*>(rec + 8 * c) = f32x4{o[0], o[1], o[2], o[3]};
        *reinterpret_cast<f32x4*>(rec + 8 * c + 4) = f32x4{o[4], o[5], o[6], o[7]};
        if (c == 0) { rec[128] = M; rec[129] = ls; }
      }
    }
    wg_barrier();
    STAMP(4);
    if (wave != 0) { STAMP_FLUSH(); return; }
    const unsigned tag = __float_as_uint(lds[L_SC + 3]);
    const __amdgpu_buffer_rsrc_t rin = rsrc(p.ws + WS_INBOX + (size_t)(b * 32 + h) * 4096, 4096);
    u32x4 gv[4];
#pragma unroll
    for (int q = 0; q < 4; ++q) gv[q] = u32x4{0u, ~tag, 0u, ~tag};
    for (unsigned spins = 0;; ++spins) {
#pragma unroll
      for (int q = 0; q < 4; ++q)
        if (gv[q][1] != tag || gv[q][3] != tag) gv[q] = __builtin_amdgcn_raw_buffer_load_b128(rin, lane * 16 + q * 1024, 0, AUX_SC1);
      int missing = 0;
#pragma unroll
      for (int q = 0; q < 4; ++q) missing += __popcll(__ballot(gv[q][1] != tag || gv[q][3] != tag));
      if (missing == 0 || spins > SPIN_CAP) break;
      if (missing > 32) __builtin_amdgcn_s_sleep(16);
    }
    STAMP(5);
    float s0 = 0.f, s1 = 0.f;
#pragma unroll
    for (int q = 0; q < 4; ++q) { s0 += __uint_as_float(gv[q][0]); s1 += __uint_as_float(gv[q][2]); }
    s0 += dppf<ROR8>(s0); s1 += dppf<ROR8>(s1);
    s0 = xrow_sum(s0); s1 = xrow_sum(s1);
    if (lane < 16) {
      const float s = lane < 8 ? s0 : s1;
      const float xb = __builtin_bit_cast(float, xfin << 16);
      const float r = s + lds[L_SC + 1] * (xb + xb * 0.f);
      p.out[b * DM + 16 * h + lane] = __builtin_bit_cast(unsigned short, (__bf16)r);
    }
    STAMP(6);
    if (h == 0 && lane == 0) __hip_atomic_store((gu32*)(p.ws + WS_EPOCH + b * 256), tag, __ATOMIC_RELAXED, __HIP_MEMORY_SCOPE_AGENT);
    STAMP_FLUSH();
    return;
  }

  const int hw = wave - NSW;
  __builtin_amdgcn_s_setprio(3);
  u32x4 msk;
#pragma unroll
  for (int w = 0; w < 4; ++w) msk[w] = (c < 8 && (c >> 1) == w) ? ((c & 1) ? 0xffff0000u : 0x0000ffffu) : 0u;
  u32x4 W[16], xp[4];
  {
    const __amdgpu_buffer_rsrc_t rq = rsrc(p.wq, DM * NH * DH * 2);
    const u32x4* xs = reinterpret_cast<const u32x4*>(p.xA + b * DM + 128 * hw + 32 * g);
#pragma unroll
    for (int i = 0; i < 4; ++i) xp[i] = xs[i];
    const int voff = (128 * hw + 32 * g) * 8192 + h * 256 + 16 * c;
#pragma unroll
    for (int i = 0; i < HD; ++i) W[i] = __builtin_amdgcn_raw_buffer_load_b128(rq, voff, i * 8192, AUX_NT);
    if (hw == 0 && lane == 0) lds[L_SC + 4] = 0.f;
    if (hw == 0 && lane < 4) lds[L_PC + lane] = 0.f;
    f32x4 d = {0, 0, 0, 0};
#pragma unroll
    for (int i = 0; i < 32; ++i) {
      d = vm_step(W[i % HD], rep16(xp[i >> 3][(i >> 1) & 3], i & 1), msk, d);
      if (i + HD < 32) W[i % HD] = __builtin_amdgcn_raw_buffer_load_b128(rq, voff, (i + HD) * 8192, AUX_NT);
      PIN();
    }
    if (c < 8) {
#pragma unroll
      for (int r = 0; r < 4; ++r) lds[L_QP + hw * 128 + 32 * g + 8 * r + c] = d[r];
    }
  }
  STAMP(1);
  __builtin_amdgcn_s_setprio(0);
  wg_barrier();
  if (hw == 2 && lane == 0) {
    const unsigned epoch = __hip_atomic_load((gu32*)(p.ws + WS_EPOCH + b * 256), __ATOMIC_RELAXED, __HIP_MEMORY_SCOPE_AGENT);
    lds[L_SC + 3] = __uint_as_float(epoch + 1u);
  }
  if (hw == 3) {
    const u32x4 xa = *reinterpret_cast<const u32x4*>(p.xA + b * DM + 8 * lane), gw = *reinterpret_cast<const u32x4*>(p.gw + 8 * lane);
    const u32x4 wr = *reinterpret_cast<const u32x4*>(p.wr + 8 * lane);
    float sg = 0.f, sr = 0.f;
#pragma unroll
    for (int k = 0; k < 4; ++k) {
      sg += rnd_bf16(bf_lo(xa[k]) * bf_lo(gw[k])) + rnd_bf16(bf_hi(xa[k]) * bf_hi(gw[k]));
      sr = dot2(xa[k], wr[k], sr);
    }
    sg = wave_sum(sg); sr = wave_sum(sr);
    const float gate = rnd_bf16(sigmoidf_(rnd_bf16(sg)));
    const float rs = rnd_bf16(sigmoidf_(rnd_bf16(sr)));
    if (lane == 0) { lds[L_SC + 0] = gate; lds[L_SC + 1] = rnd_bf16(1.f - gate); lds[L_SC + 2] = rs; }
  }
  f32x4 dy = {0, 0, 0, 0};
  const int hs = !FMOVE ? h : (h == 7 || h == 15 || h == 23) ? 99 : (h >= 28 && h <= 30) ? 8 * (h - 28) + 7 : h;
  if (hs < FF / 64) {
    {
      f32x4 d = {0, 0, 0, 0};
      const unsigned short* wbase = (c < 8 ? p.wg : p.wu) + 64 * hs + 8 * (c & 7) + (size_t)(128 * hw + 32 * g) * FF;
#pragma unroll
      for (int i = 0; i < HD2; ++i) W[i] = ldw(wbase + (size_t)i * FF);
#pragma unroll
      for (int i = 0; i < 32; ++i) {
        d = vm_step(W[i % HD2], rep16(xp[i >> 3][(i >> 1) & 3], i & 1), msk, d);
        if (i + HD2 < 32) W[i % HD2] = ldw(wbase + (size_t)(i + HD2) * FF);
        PIN();
      }
      if (c < 8) {
#pragma unroll
        for (int r = 0; r < 4; ++r) lds[L_GU + hw * 128 + 32 * g + 8 * r + c] = d[r];
      }
      asm volatile("s_waitcnt lgkmcnt(0)" ::: "memory");
      if (lane == 0) __hip_atomic_fetch_add((LAS unsigned*)&lds[L_SC + 4], 1u, __ATOMIC_RELAXED, __HIP_MEMORY_SCOPE_WORKGROUP);
    }
    const __amdgpu_buffer_rsrc_t rd_ = rsrc(p.wd, FF * DM * 2);
    const int voff = (64 * hs + 16 * g) * 1024 + 256 * hw + 16 * c;
#pragma unroll
    for (int i = 0; i < 16; ++i) {
      W[i] = __builtin_amdgcn_raw_buffer_load_b128(rd_, voff, i * 1024, AUX_NT);
      if ((i % HD2) == HD2 - 1) asm volatile("s_waitcnt vmcnt(0)" ::: "memory");
    }
    while (__hip_atomic_load((LAS unsigned*)&lds[L_SC + 4], __ATOMIC_RELAXED, __HIP_MEMORY_SCOPE_WORKGROUP) < 4u) __builtin_amdgcn_s_sleep(4);
    asm volatile("" ::: "memory");
    float gt = 0.f, up = 0.f;
#pragma unroll
    for (int w = 0; w < 4; ++w) { gt += lds[L_GU + w * 128 + lane]; up += lds[L_GU + w * 128 + 64 + lane]; }
    gt = rnd_bf16(gt); up = rnd_bf16(up);
    const float si = rnd_bf16(gt * sigmoidf_(gt));
    lds[L_H + hw * 64 + lane] = __uint_as_float(pk_bf16(si * up, si * up));
    asm volatile("s_waitcnt lgkmcnt(0)" ::: "memory");
    u32x4 hp[4];
#pragma unroll
    for (int i = 0; i < 4; ++i) hp[i] = *reinterpret_cast<const u32x4*>(&lds[L_H + hw * 64 + 16 * g + 4 * i]);
#pragma unroll
    for (int i = 0; i < 16; ++i) dy = vm_step(W[i], hp[i >> 2][i & 3], msk, dy);
  }
  if (hw == 2) {
    while (__hip_atomic_load((LAS unsigned*)&lds[L_PC + 1], __ATOMIC_RELAXED, __HIP_MEMORY_SCOPE_WORKGROUP) == 0u && __builtin_amdgcn_s_memrealtime() - t_entry < T_READ) __builtin_amdgcn_s_sleep(16);
    const unsigned tag = __float_as_uint(lds[L_SC + 3]);
    const unsigned ta = __float_as_uint(lds[L_PC + 0]), tb = __float_as_uint(lds[L_PC + 1]);
    if (lane == 0 && tb != 0u)
      __hip_atomic_store((GAS unsigned long long*)(p.ws + WS_PACE + b * 256 + h * 8), ((unsigned long long)tag << 32) | (tb << 16) | ta, __ATOMIC_RELAXED, __HIP_MEMORY_SCOPE_AGENT);
    while (__builtin_amdgcn_s_memrealtime() - t_entry < T_READ) __builtin_amdgcn_s_sleep(16);
    typedef unsigned u32x16 __attribute__((ext_vector_type(16)));
    const __attribute__((address_space(4))) u32x16* sp = (const __attribute__((address_space(4))) u32x16*)(p.ws + WS_PACE + b * 256);
    unsigned sa = 0, sb = 0, n = 0;
#pragma unroll 1
    for (int q = 0; q < 4; ++q) {
      const u32x16 v = sp[q];
#pragma unroll
      for (int k = 0; k < 8; ++k) {
        const bool ok = v[2 * k + 1] == tag;
        sa += ok ? (v[2 * k] & 0xffffu) : 0u; sb += ok ? (v[2 * k] >> 16) : 0u; n += ok ? 1u : 0u;
      }
    }
    if (n >= 16u) {
      sa /= n; sb /= n;
      const unsigned slope = ((sb - sa) * ADAPT_Q) / (256u * (PB - PA));
      const unsigned at_pc = sb + ((sb - sa) * (PC - PB)) / (PB - PA);
      const unsigned line_pc = PACE_T0 + PC * PACE_DT, line_end = PACE_T0 + (TILES - 2) * PACE_DT;
      const unsigned end = at_pc + slope * (TILES - 2 - PC);
      if (end > line_end + ADAPT_M) {
        const unsigned base = min(max(at_pc - ADAPT_M, line_pc), line_pc + 2000u);
        const unsigned nslope = (end - ADAPT_M - min(base, end - ADAPT_M) + (TILES - 2 - PC) / 2) / (TILES - 2 - PC);
        if (lane == 0) { lds[L_PC + 2] = __uint_as_float(base); lds[L_PC + 3] = __uint_as_float(min(max(nslope, 100u), 200u)); }
      }
    }
  }
  STAMP(2);
  u32x4 WB[8][4];
  {
    const __amdgpu_buffer_rsrc_t ro = rsrc(p.wo, NH * DH * DM * 2);
    const int voff = (128 * h + g) * 1024 + 256 * hw + 16 * c;
    float* st = &lds[L_ST + hw * 2048];
#pragma unroll
    for (int s = 0; s < 4; ++s) {
#pragma unroll
      for (int hf = 0; hf < 2; ++hf) {
#pragma unroll
        for (int i = 0; i < 4; ++i) W[i] = __builtin_amdgcn_raw_buffer_load_b128(ro, voff, (32 * s + 16 * hf + 4 * i) * 1024, AUX_NT);
#pragma unroll
        for (int i = 0; i < 4; ++i) *reinterpret_cast<u32x4*>(&st[(16 * hf + 4 * i + g) * 64 + 4 * c]) = W[i];
      }
      asm volatile("s_waitcnt lgkmcnt(0)" ::: "memory");
#pragma unroll
      for (int t = 0; t < 8; ++t) {
        const float* a0 = &st[(8 * g + (c >> 2)) * 64 + 8 * t + 2 * (c & 3)];
        const s16x4 u0 = __builtin_amdgcn_ds_read_tr16_b64_v4i16((LAS s16x4*)a0);
        const s16x4 u1 = __builtin_amdgcn_ds_read_tr16_b64_v4i16((LAS s16x4*)(a0 + 4 * 64));
        const u32x2 x0 = __builtin_bit_cast(u32x2, u0), x1 = __builtin_bit_cast(u32x2, u1);
        WB[t][s] = u32x4{x0[0], x0[1], x1[0], x1[1]};
      }
      asm volatile("s_waitcnt lgkmcnt(0)" ::: "memory");
    }
  }
  STAMP(3);
  wg_barrier();
  STAMP(4);
  {
    float mw[NSW], lw[NSW];
#pragma unroll
    for (int w = 0; w < NSW; ++w) { mw[w] = lds[L_MRG + w * 132 + 128]; lw[w] = lds[L_MRG + w * 132 + 129]; }
    float M = mw[0];
#pragma unroll
    for (int w = 1; w < NSW; ++w) M = fmaxf(M, mw[w]);
    float L = 0.f, o0 = 0.f, o1 = 0.f;
#pragma unroll
    for (int w = 0; w < NSW; ++w) {
      const float wt = __builtin_amdgcn_exp2f(mw[w] - M);
      const u32x2 ov = *reinterpret_cast<const u32x2*>(&lds[L_MRG + w * 132 + 32 * g + 2 * c]);
      L = fmaf(wt, lw[w], L); o0 = fmaf(wt, __uint_as_float(ov[0]), o0); o1 = fmaf(wt, __uint_as_float(ov[1]), o1);
    }
    const float inv = __builtin_amdgcn_rcpf(L);
    float* ob = &lds[L_H + hw * 64];
    ob[16 * g + c] = __uint_as_float(pk_bf16(o0 * inv, o1 * inv));
    asm volatile("s_waitcnt lgkmcnt(0)" ::: "memory");
    u32x4 oa[4];
#pragma unroll
    for (int s = 0; s < 4; ++s) oa[s] = *reinterpret_cast<const u32x4*>(&ob[16 * s + 4 * g]);
    f32x4 d[8];
#pragma unroll
    for (int t = 0; t < 8; ++t) d[t] = f32x4{0, 0, 0, 0};
#pragma unroll
    for (int s = 0; s < 4; ++s)
#pragma unroll
      for (int t = 0; t < 8; ++t) d[t] = mfma(oa[s], WB[t][s], d[t]);
    unsigned m1 = (g & 1) ? ~0u : 0u, m2 = (g & 2) ? ~0u : 0u;
    asm volatile("" : "+v"(m1), "+v"(m2));
    const float gate = lds[L_SC + 0], yc = lds[L_SC + 1] * lds[L_SC + 2];
    const unsigned tag = __float_as_uint(lds[L_SC + 3]);
    const __amdgpu_buffer_rsrc_t rin = rsrc(p.ws + WS_INBOX + (size_t)b * 32 * 4096, 32 * 4096);
#pragma unroll
    for (int pi = 0; pi < 2; ++pi) {
      const unsigned lo = (m1 & __float_as_uint(d[2 + pi][0])) | (~m1 & __float_as_uint(d[pi][0]));
      const unsigned hi = (m1 & __float_as_uint(d[6 + pi][0])) | (~m1 & __float_as_uint(d[4 + pi][0]));
      const float a0 = __uint_as_float((m2 & hi) | (~m2 & lo));
      const float a1 = dppf<ROR8>(a0);
      if (c < 8) {
        const float v0 = gate * a0 + yc * dy[2 * pi], v1 = gate * a1 + yc * dy[2 * pi + 1];
        const u32x4 gr = {__float_as_uint(v0), tag, __float_as_uint(v1), tag};
        __builtin_amdgcn_raw_buffer_store_b128(gr, rin, (8 * hw + 2 * g + pi) * 4096 + h * 128 + 16 * c, 0, AUX_SC1);
      }
    }
    STAMP(5);
    STAMP_FLUSH();
  }
}

extern "C" void kernel_launch(void* const* d_in, const int* in_sizes, int n_in, void* d_out, int out_size, void* d_ws, size_t ws_size,
                              hipStream_t stream) {
  Params p;
  p.xA = (const unsigned short*)d_in[0]; p.K = (const unsigned short*)d_in[1]; p.V = (const unsigned short*)d_in[2];
  p.wq = (const unsigned short*)d_in[3]; p.wo = (const unsigned short*)d_in[4];
  p.wr = (const unsigned short*)d_in[6]; p.wg = (const unsigned short*)d_in[7]; p.wu = (const unsigned short*)d_in[8];
  p.wd = (const unsigned short*)d_in[9]; p.gw = (const unsigned short*)d_in[10];
  p.out = (unsigned short*)d_out; p.ws = (unsigned char*)d_ws;
  fused_decode_kernel<<<NB * NH, NT, 0, stream>>>(p);
}
```

```cpp
#include <hip/hip_runtime.h>
#include <hip/hip_bf16.h>

typedef unsigned u32x4 __attribute__((ext_vector_type(4)));
typedef unsigned u32x2 __attribute__((ext_vector_type(2)));
typedef float f32x4 __attribute__((ext_vector_type(4)));
typedef short s16x4 __attribute__((ext_vector_type(4)));
typedef __bf16 bf16x2 __attribute__((ext_vector_type(2)));
typedef __bf16 bf16x8 __attribute__((ext_vector_type(8)));
#define GAS __attribute__((address_space(1)))
#define LAS __attribute__((address_space(3)))
typedef GAS unsigned gu32;

constexpr int NB = 8, NH = 32, SK = 4096, DH = 128, DM = 512, FF = 1792;
constexpr int NSW = 4;
constexpr int NT = (NSW + 4) * 64;
constexpr int TILES = SK / 16 / NSW;
constexpr int BURST = 3;
constexpr int HD = 8;
constexpr int HD2 = 4;
constexpr unsigned PACE_DT = 117, PACE_T0 = 160, PACE_M = 100, PACE_Q = 248, T_READ = 3300;
constexpr int PA = 8, PB = 22, PC = 30;
constexpr int CODE_PF = 15360;
constexpr float C2 = 0.08838834764831845f * 1.4426950408889634f;
constexpr int RSRC3 = 0x00020000;
constexpr int AUX_NT = 2, AUX_SC1 = 16;
constexpr int WS_EPOCH = 0, WS_INBOX = 4096, WS_PACE = WS_INBOX + (1 << 20);
constexpr unsigned SPIN_CAP = 1u << 16;

struct Params {
  const unsigned short *K, *V, *xA, *wq, *wo, *wr, *wg, *wu, *wd, *gw;
  unsigned short* out;
  unsigned char* ws;
};

constexpr int L_QP = 0;
constexpr int L_MRG = L_QP + 512;
constexpr int L_GU = L_MRG + NSW * 132;
constexpr int L_H = L_GU + 512;
constexpr int L_SC = L_H + 256;
constexpr int L_PC = L_SC + 8;
constexpr int L_ST = L_PC + 4;
constexpr int L_END = L_ST + 4 * 2048;

__device__ __forceinline__ float dot2(unsigned a, unsigned b, float c) {
  return __builtin_amdgcn_fdot2_f32_bf16(__builtin_bit_cast(bf16x2, a), __builtin_bit_cast(bf16x2, b), c, false);
}
__device__ __forceinline__ unsigned pk_bf16(float lo, float hi) {
  bf16x2 v = {(__bf16)lo, (__bf16)hi};
  return __builtin_bit_cast(unsigned, v);
}
__device__ __forceinline__ float rnd_bf16(float x) { return (float)(__bf16)x; }
__device__ __forceinline__ float bf_lo(unsigned u) { return __builtin_bit_cast(float, u << 16); }
__device__ __forceinline__ float bf_hi(unsigned u) { return __builtin_bit_cast(float, u & 0xffff0000u); }
template <int CTRL> __device__ __forceinline__ float dppf(float x) {
  return __builtin_bit_cast(float, __builtin_amdgcn_mov_dpp(__builtin_bit_cast(int, x), CTRL, 0xf, 0xf, true));
}
template <int CTRL> __device__ __forceinline__ unsigned dppu(unsigned x) {
  return (unsigned)__builtin_amdgcn_mov_dpp((int)x, CTRL, 0xf, 0xf, true);
}
constexpr int XOR1 = 0xB1, XOR2 = 0x4E, XOR7 = 0x141, ROR8 = 0x128, ROR4 = 0x124, BCAST = 0x150;
__device__ __forceinline__ float xrow_sum(float x) {
  auto s = __builtin_amdgcn_permlane16_swap(__float_as_uint(x), __float_as_uint(x), false, false);
  x = __uint_as_float(s[0]) + __uint_as_float(s[1]);
  auto t = __builtin_amdgcn_permlane32_swap(__float_as_uint(x), __float_as_uint(x), false, false);
  return __uint_as_float(t[0]) + __uint_as_float(t[1]);
}
__device__ __forceinline__ float xrow_max(float x) {
  auto s = __builtin_amdgcn_permlane16_swap(__float_as_uint(x), __float_as_uint(x), false, false);
  x = fmaxf(__uint_as_float(s[0]), __uint_as_float(s[1]));
  auto t = __builtin_amdgcn_permlane32_swap(__float_as_uint(x), __float_as_uint(x), false, false);
  return fmaxf(__uint_as_float(t[0]), __uint_as_float(t[1]));
}
__device__ __forceinline__ float wave_sum(float x) {
  x += dppf<XOR1>(x); x += dppf<XOR2>(x); x += dppf<XOR7>(x); x += dppf<ROR8>(x);
  return xrow_sum(x);
}
__device__ __forceinline__ f32x4 mfma(u32x4 a, u32x4 b, f32x4 c) {
  return __builtin_amdgcn_mfma_f32_16x16x32_bf16(__builtin_bit_cast(bf16x8, a), __builtin_bit_cast(bf16x8, b), c, 0, 0, 0);
}
__device__ __forceinline__ void wg_barrier() {
  asm volatile("s_waitcnt lgkmcnt(0)" ::: "memory");
  __builtin_amdgcn_s_barrier();
  asm volatile("" ::: "memory");
}
__device__ __forceinline__ float sigmoidf_(float x) { return 1.f / (1.f + __expf(-x)); }
__device__ __forceinline__ float safe_max(float m) { return m == -INFINITY ? 0.f : m; }
__device__ __forceinline__ __amdgpu_buffer_rsrc_t rsrc(const void* p, int bytes) {
  return __builtin_amdgcn_make_buffer_rsrc(const_cast<void*>(p), 0, bytes, RSRC3);
}
__device__ __forceinline__ f32x4 vm_step(u32x4 w, unsigned pp, const u32x4& msk, f32x4 d) {
  u32x4 bq = {pp & msk[0], pp & msk[1], pp & msk[2], pp & msk[3]};
  return mfma(w, bq, d);
}
__device__ __forceinline__ unsigned rep16(unsigned pair, int odd) {
  return odd ? __builtin_amdgcn_perm(pair, pair, 0x03020302u) : __builtin_amdgcn_perm(pair, pair, 0x01000100u);
}
__device__ __forceinline__ u32x4 ldw(const unsigned short* p) { return __builtin_nontemporal_load(reinterpret_cast<const u32x4*>(p)); }
__device__ __forceinline__ unsigned lds_word(const float* p) { return __hip_atomic_load((LAS unsigned*)p, __ATOMIC_RELAXED, __HIP_MEMORY_SCOPE_WORKGROUP); }
#define PIN() __builtin_amdgcn_sched_barrier(0)

__global__ void __launch_bounds__(NT, NT / 256) fused_decode_kernel(Params p) {
  __shared__ __attribute__((aligned(16))) float lds[L_END];
  const int tid = threadIdx.x, lane = tid & 63, wave = __builtin_amdgcn_readfirstlane(tid >> 6);
  const int g = lane >> 4, c = lane & 15;
  const int bid = blockIdx.x, h = (bid & 7) * 4 + ((bid >> 3) & 3), b = bid >> 5;
  const unsigned long long t_entry = __builtin_amdgcn_s_memrealtime();
  unsigned long long pc0;
  asm volatile("s_getpc_b64 %0" : "=s"(pc0));
  const unsigned short *Kp = p.K, *Vp = p.V;
  asm volatile("" : "+s"(Kp), "+s"(Vp));

  if (__builtin_expect(wave < NSW, 1)) {
    const size_t bh = (size_t)(b * NH + h) * SK * DH;
    const __amdgpu_buffer_rsrc_t rk = rsrc(Kp + bh, SK * DH * 2), rv = rsrc(Vp + bh, SK * DH * 2);
    const int voff = lane * 16;
    unsigned cpf[2] = {0u, 0u};
    static_assert(CODE_PF <= 16384, "two loads; CODE_PF must not exceed the length of the kernel's code");
    if (wave == NSW - 1) {
      const unsigned char* pc = (const unsigned char*)(pc0 & ~127ull);
#pragma unroll
      for (int i = 0; i < (CODE_PF + 8191) / 8192; ++i)
        if (i * 8192 + lane * 128 < CODE_PF) cpf[i] = *(const GAS unsigned*)(pc + i * 8192 + lane * 128);
    }
    auto ldk = [&](int i, int j) { return __builtin_amdgcn_raw_buffer_load_b128(rk, voff, (i * NSW + wave) * 4096 + j * 1024, AUX_NT); };
    auto ldv = [&](int i, int j) { return __builtin_amdgcn_raw_buffer_load_b128(rv, voff, (i * NSW + wave) * 4096 + j * 1024, AUX_NT); };
    u32x4 RK[4], RV[4], EK[BURST][4], EV[BURST][4];
#pragma unroll
    for (int j = 0; j < 4; ++j) RK[j] = ldk(BURST, j);
#pragma unroll
    for (int j = 0; j < 4; ++j) RV[j] = ldv(BURST, j);
#pragma unroll
    for (int e = 0; e < BURST; ++e) {
#pragma unroll
      for (int j = 0; j < 4; ++j) EK[e][j] = ldk(e, j);
#pragma unroll
      for (int j = 0; j < 4; ++j) EV[e][j] = ldv(e, j);
    }
    wg_barrier();
    asm volatile("" :: "v"(cpf[0]), "v"(cpf[1]));
    unsigned qp[4];
    {
      f32x4 q0 = {0, 0, 0, 0}, q1 = {0, 0, 0, 0};
#pragma unroll
      for (int w = 0; w < 4; ++w) {
        q0 += *reinterpret_cast<const f32x4*>(&lds[L_QP + w * 128 + 8 * c]);
        q1 += *reinterpret_cast<const f32x4*>(&lds[L_QP + w * 128 + 8 * c + 4]);
      }
      qp[0] = pk_bf16(q0[0], q0[1]); qp[1] = pk_bf16(q0[2], q0[3]); qp[2] = pk_bf16(q1[0], q1[1]); qp[3] = pk_bf16(q1[2], q1[3]);
    }
    float m = -INFINITY, ms = 0.f, l = 0.f, acc[8];
#pragma unroll
    for (int k = 0; k < 8; ++k) acc[k] = 0.f;
    const bool b8 = c & 8, b4 = c & 4;

    auto process = [&](u32x4 (&TK)[4], u32x4 (&TV)[4], bool refill, int inext) {
      float x[4];
#pragma unroll
      for (int j = 0; j < 4; ++j) {
        float d = dot2(TK[j][0], qp[0], 0.f);
        d = dot2(TK[j][1], qp[1], d); d = dot2(TK[j][2], qp[2], d); d = dot2(TK[j][3], qp[3], d);
        x[j] = d;
        if (refill) { TK[j] = ldk(inext, j); PIN(); }
      }
      const float a0 = x[0] + dppf<ROR8>(x[0]), a1 = x[1] + dppf<ROR8>(x[1]), a2 = x[2] + dppf<ROR8>(x[2]), a3 = x[3] + dppf<ROR8>(x[3]);
      const float y0 = b8 ? a2 : a0, y1 = b8 ? a3 : a1;
      const float z0 = y0 + dppf<XOR7>(y0), z1 = y1 + dppf<XOR7>(y1);
      float z = b4 ? z1 : z0;
      z += dppf<XOR2>(z); z += dppf<XOR1>(z);
      const float sc = z * C2;
      float pm = fmaxf(sc, dppf<ROR4>(sc)); pm = fmaxf(pm, dppf<ROR8>(pm));
      if (__any(pm > m)) {
        const float mn = fmaxf(m, pm), msn = safe_max(mn), al = __builtin_amdgcn_exp2f(m == -INFINITY ? -INFINITY : m - msn);
        l *= al;
#pragma unroll
        for (int k = 0; k < 8; ++k) acc[k] *= al;
        m = mn; ms = msn;
      }
      const float pe = __builtin_amdgcn_exp2f(sc - ms);
      l += pe;
      const unsigned plo = pk_bf16(pe, 0.f), phi = plo << 16;
#define PV(j) { const unsigned lo_ = dppu<BCAST + 4 * (j)>(plo), hi_ = dppu<BCAST + 4 * (j)>(phi); \
        _Pragma("unroll") for (int k = 0; k < 4; ++k) { acc[2 * k] = dot2(TV[j][k], lo_, acc[2 * k]); acc[2 * k + 1] = dot2(TV[j][k], hi_, acc[2 * k + 1]); } \
        if (refill) { TV[j] = ldv(inext, j); PIN(); } }
      PV(0) PV(1) PV(2) PV(3)
#undef PV
      asm volatile("" : "+v"(acc[0]), "+v"(acc[1]), "+v"(acc[2]), "+v"(acc[3]), "+v"(acc[4]), "+v"(acc[5]), "+v"(acc[6]), "+v"(acc[7]));
    };
    process(RK, RV, true, BURST + 1);
#pragma unroll
    for (int e = 0; e < BURST; ++e) process(EK[e], EV[e], false, 0);
    unsigned long long tnext = t_entry + PACE_T0 + BURST * PACE_DT;
    unsigned dt = PACE_DT;
    for (int i = BURST + 1; i < TILES - 1; ++i) {
      tnext += dt;
      unsigned long long now = __builtin_amdgcn_s_memrealtime();
      while ((long long)(now - tnext) < 0) { __builtin_amdgcn_s_sleep(1); now = __builtin_amdgcn_s_memrealtime(); }
      if (wave == 0 && (i == PA || i == PB)) { if (lane == 0) __hip_atomic_store((LAS unsigned*)&lds[L_PC + (i == PB)], (unsigned)(now - t_entry), __ATOMIC_RELAXED, __HIP_MEMORY_SCOPE_WORKGROUP); }
      if (i == PC) {
        const unsigned nl = __builtin_amdgcn_readfirstlane(lds_word(&lds[L_PC + 2]));
        if (nl != 0u) { tnext = t_entry + (nl & 0xffffu); dt = nl >> 16; }
      }
      process(RK, RV, true, i + 1);
    }
    process(RK, RV, false, 0);
    unsigned xfin = 0;
    if (wave == 0 && lane < 16) xfin = p.xA[b * DM + 16 * h + lane];
    {
      const float M = xrow_max(m), sc = __builtin_amdgcn_exp2f(m - safe_max(M));
      float ls = l * sc;
      ls += dppf<ROR4>(ls); ls += dppf<ROR8>(ls);
      ls = xrow_sum(ls);
      float o[8];
#pragma unroll
      for (int k = 0; k < 8; ++k) o[k] = xrow_sum(acc[k] * sc);
      float* rec = &lds[L_MRG + wave * 132];
      if (g == 0) {
        *reinterpret_cast<f32x4*>(rec + 8 * c) = f32x4{o[0], o[1], o[2], o[3]};
        *reinterpret_cast<f32x4*>(rec + 8 * c + 4) = f32x4{o[4], o[5], o[6], o[7]};
        if (c == 0) { rec[128] = M; rec[129] = ls; }
      }
    }
    wg_barrier();
    if (wave != 0) return;
    const unsigned tag = __float_as_uint(lds[L_SC + 3]);
    const __amdgpu_buffer_rsrc_t rin = rsrc(p.ws + WS_INBOX + (size_t)(b * 32 + h) * 4096, 4096);
    u32x4 gv[4];
#pragma unroll
    for (int q = 0; q < 4; ++q) gv[q] = u32x4{0u, ~tag, 0u, ~tag};
    for (unsigned spins = 0;; ++spins) {
#pragma unroll
      for (int q = 0; q < 4; ++q)
        if (gv[q][1] != tag || gv[q][3] != tag) gv[q] = __builtin_amdgcn_raw_buffer_load_b128(rin, lane * 16 + q * 1024, 0, AUX_SC1);
      int missing = 0;
#pragma unroll
      for (int q = 0; q < 4; ++q) missing += __popcll(__ballot(gv[q][1] != tag || gv[q][3] != tag));
      if (missing == 0 || spins > SPIN_CAP) break;
      if (missing > 32) __builtin_amdgcn_s_sleep(16);
    }
    float s0 = 0.f, s1 = 0.f;
#pragma unroll
    for (int q = 0; q < 4; ++q) { s0 += __uint_as_float(gv[q][0]); s1 += __uint_as_float(gv[q][2]); }
    s0 += dppf<ROR8>(s0); s1 += dppf<ROR8>(s1);
    s0 = xrow_sum(s0); s1 = xrow_sum(s1);
    if (lane < 16) {
      const float s = lane < 8 ? s0 : s1;
      const float xb = __builtin_bit_cast(float, xfin << 16);
      const float r = s + lds[L_SC + 1] * (xb + xb * 0.f);
      p.out[b * DM + 16 * h + lane] = __builtin_bit_cast(unsigned short, (__bf16)r);
    }
    if (h == 0 && lane == 0) __hip_atomic_store((gu32*)(p.ws + WS_EPOCH + b * 256), tag, __ATOMIC_RELAXED, __HIP_MEMORY_SCOPE_AGENT);
    return;
  }

  const int hw = wave - NSW;
  __builtin_amdgcn_s_setprio(3);
  u32x4 msk;
#pragma unroll
  for (int w = 0; w < 4; ++w) msk[w] = (c < 8 && (c >> 1) == w) ? ((c & 1) ? 0xffff0000u : 0x0000ffffu) : 0u;
  u32x4 W[16], xp[4];
  {
    const __amdgpu_buffer_rsrc_t rq = rsrc(p.wq, DM * NH * DH * 2);
    const u32x4* xs = reinterpret_cast<const u32x4*>(p.xA + b * DM + 128 * hw + 32 * g);
#pragma unroll
    for (int i = 0; i < 4; ++i) xp[i] = xs[i];
    const int voff = (128 * hw + 32 * g) * 8192 + h * 256 + 16 * c;
#pragma unroll
    for (int i = 0; i < HD; ++i) W[i] = __builtin_amdgcn_raw_buffer_load_b128(rq, voff, i * 8192, AUX_NT);
    if (hw == 0 && lane == 0) lds[L_SC + 4] = 0.f;
    if (hw == 0 && lane < 4) lds[L_PC + lane] = 0.f;
    f32x4 d = {0, 0, 0, 0};
#pragma unroll
    for (int i = 0; i < 32; ++i) {
      d = vm_step(W[i % HD], rep16(xp[i >> 3][(i >> 1) & 3], i & 1), msk, d);
      if (i + HD < 32) W[i % HD] = __builtin_amdgcn_raw_buffer_load_b128(rq, voff, (i + HD) * 8192, AUX_NT);
      PIN();
    }
    if (c < 8) {
#pragma unroll
      for (int r = 0; r < 4; ++r) lds[L_QP + hw * 128 + 32 * g + 8 * r + c] = d[r];
    }
  }
  __builtin_amdgcn_s_setprio(0);
  wg_barrier();
  if (hw == 2 && lane == 0) {
    const unsigned epoch = __hip_atomic_load((gu32*)(p.ws + WS_EPOCH + b * 256), __ATOMIC_RELAXED, __HIP_MEMORY_SCOPE_AGENT);
    lds[L_SC + 3] = __uint_as_float(epoch + 1u);
  }
  if (hw == 3) {
    const u32x4 xa = *reinterpret_cast<const u32x4*>(p.xA + b * DM + 8 * lane), gw = *reinterpret_cast<const u32x4*>(p.gw + 8 * lane);
    const u32x4 wr = *reinterpret_cast<const u32x4*>(p.wr + 8 * lane);
    float sg = 0.f, sr = 0.f;
#pragma unroll
    for (int k = 0; k < 4; ++k) {
      sg += rnd_bf16(bf_lo(xa[k]) * bf_lo(gw[k])) + rnd_bf16(bf_hi(xa[k]) * bf_hi(gw[k]));
      sr = dot2(xa[k], wr[k], sr);
    }
    sg = wave_sum(sg); sr = wave_sum(sr);
    const float gate = rnd_bf16(sigmoidf_(rnd_bf16(sg)));
    const float rs = rnd_bf16(sigmoidf_(rnd_bf16(sr)));
    if (lane == 0) { lds[L_SC + 0] = gate; lds[L_SC + 1] = rnd_bf16(1.f - gate); lds[L_SC + 2] = rs; }
  }
  f32x4 dy = {0, 0, 0, 0};
  const int hs = (h == 7 || h == 15 || h == 23) ? NH : (h >= 28 && h <= 30) ? 8 * (h - 28) + 7 : h;
  if (hs < FF / 64) {
    {
      f32x4 d = {0, 0, 0, 0};
      const unsigned short* wbase = (c < 8 ? p.wg : p.wu) + 64 * hs + 8 * (c & 7) + (size_t)(128 * hw + 32 * g) * FF;
#pragma unroll
      for (int i = 0; i < HD2; ++i) W[i] = ldw(wbase + (size_t)i * FF);
#pragma unroll
      for (int i = 0; i < 32; ++i) {
        d = vm_step(W[i % HD2], rep16(xp[i >> 3][(i >> 1) & 3], i & 1), msk, d);
        if (i + HD2 < 32) W[i % HD2] = ldw(wbase + (size_t)(i + HD2) * FF);
        PIN();
      }
      if (c < 8) {
#pragma unroll
        for (int r = 0; r < 4; ++r) lds[L_GU + hw * 128 + 32 * g + 8 * r + c] = d[r];
      }
      asm volatile("s_waitcnt lgkmcnt(0)" ::: "memory");
      if (lane == 0) __hip_atomic_fetch_add((LAS unsigned*)&lds[L_SC + 4], 1u, __ATOMIC_RELAXED, __HIP_MEMORY_SCOPE_WORKGROUP);
    }
    const __amdgpu_buffer_rsrc_t rd_ = rsrc(p.wd, FF * DM * 2);
    const int voff = (64 * hs + 16 * g) * 1024 + 256 * hw + 16 * c;
#pragma unroll
    for (int i = 0; i < 16; ++i) {
      W[i] = __builtin_amdgcn_raw_buffer_load_b128(rd_, voff, i * 1024, AUX_NT);
      if ((i % HD2) == HD2 - 1) asm volatile("s_waitcnt vmcnt(0)" ::: "memory");
    }
    while (lds_word(&lds[L_SC + 4]) < 4u) __builtin_amdgcn_s_sleep(4);
    asm volatile("" ::: "memory");
    float gt = 0.f, up = 0.f;
#pragma unroll
    for (int w = 0; w < 4; ++w) { gt += lds[L_GU + w * 128 + lane]; up += lds[L_GU + w * 128 + 64 + lane]; }
    gt = rnd_bf16(gt); up = rnd_bf16(up);
    const float si = rnd_bf16(gt * sigmoidf_(gt));
    lds[L_H + hw * 64 + lane] = __uint_as_float(pk_bf16(si * up, si * up));
    asm volatile("s_waitcnt lgkmcnt(0)" ::: "memory");
    u32x4 hp[4];
#pragma unroll
    for (int i = 0; i < 4; ++i) hp[i] = *reinterpret_cast<const u32x4*>(&lds[L_H + hw * 64 + 16 * g + 4 * i]);
#pragma unroll
    for (int i = 0; i < 16; ++i) dy = vm_step(W[i], hp[i >> 2][i & 3], msk, dy);
  }
  if (hw == 2) {
    while (lds_word(&lds[L_PC + 1]) == 0u && __builtin_amdgcn_s_memrealtime() - t_entry < T_READ) __builtin_amdgcn_s_sleep(16);
    asm volatile("" ::: "memory");
    const unsigned tag = __float_as_uint(lds[L_SC + 3]);
    const unsigned ta = lds_word(&lds[L_PC + 0]), tb = lds_word(&lds[L_PC + 1]);
    if (lane == 0 && tb != 0u)
      __hip_atomic_store((GAS unsigned long long*)(p.ws + WS_PACE + b * 256 + h * 8), ((unsigned long long)tag << 32) | (tb << 16) | ta, __ATOMIC_RELAXED, __HIP_MEMORY_SCOPE_AGENT);
    while (__builtin_amdgcn_s_memrealtime() - t_entry < T_READ) __builtin_amdgcn_s_sleep(16);
    typedef unsigned u32x16 __attribute__((ext_vector_type(16)));
    const __attribute__((address_space(4))) u32x16* sp = (const __attribute__((address_space(4))) u32x16*)(p.ws + WS_PACE + b * 256);
    asm volatile("" : "+s"(sp));
    unsigned sa = 0, sb = 0, n = 0;
#pragma unroll 1
    for (int q = 0; q < 4; ++q) {
      const u32x16 v = sp[q];
#pragma unroll
      for (int k = 0; k < 8; ++k) {
        const bool ok = v[2 * k + 1] == tag;
        sa += ok ? (v[2 * k] & 0xffffu) : 0u; sb += ok ? (v[2 * k] >> 16) : 0u; n += ok ? 1u : 0u;
      }
    }
    if (n >= 16u) {
      sa /= n; sb /= n;
      const unsigned slope = ((sb - sa) * PACE_Q) / (256u * (PB - PA));
      const unsigned at_pc = sb + ((sb - sa) * (PC - PB)) / (PB - PA);
      const unsigned line_pc = PACE_T0 + PC * PACE_DT, line_end = PACE_T0 + (TILES - 2) * PACE_DT;
      const unsigned end = at_pc + slope * (TILES - 2 - PC);
      if (end > line_end + PACE_M) {
        const unsigned base = min(max(at_pc - PACE_M, line_pc), line_pc + 2000u);
        const unsigned nslope = (end - PACE_M - min(base, end - PACE_M) + (TILES - 2 - PC) / 2) / (TILES - 2 - PC);
        if (lane == 0) __hip_atomic_store((LAS unsigned*)&lds[L_PC + 2], base | (min(max(nslope, 100u), 200u) << 16), __ATOMIC_RELAXED, __HIP_MEMORY_SCOPE_WORKGROUP);
      }
    }
  }
  u32x4 WB[8][4];
  {
    const __amdgpu_buffer_rsrc_t ro = rsrc(p.wo, NH * DH * DM * 2);
    const int voff = (128 * h + g) * 1024 + 256 * hw + 16 * c;
    float* st = &lds[L_ST + hw * 2048];
#pragma unroll
    for (int s = 0; s < 4; ++s) {
#pragma unroll
      for (int hf = 0; hf < 2; ++hf) {
#pragma unroll
        for (int i = 0; i < 4; ++i) W[i] = __builtin_amdgcn_raw_buffer_load_b128(ro, voff, (32 * s + 16 * hf + 4 * i) * 1024, AUX_NT);
#pragma unroll
        for (int i = 0; i < 4; ++i) *reinterpret_cast<u32x4*>(&st[(16 * hf + 4 * i + g) * 64 + 4 * c]) = W[i];
      }
      asm volatile("s_waitcnt lgkmcnt(0)" ::: "memory");
#pragma unroll
      for (int t = 0; t < 8; ++t) {
        const float* a0 = &st[(8 * g + (c >> 2)) * 64 + 8 * t + 2 * (c & 3)];
        const s16x4 u0 = __builtin_amdgcn_ds_read_tr16_b64_v4i16((LAS s16x4*)a0);
        const s16x4 u1 = __builtin_amdgcn_ds_read_tr16_b64_v4i16((LAS s16x4*)(a0 + 4 * 64));
        const u32x2 x0 = __builtin_bit_cast(u32x2, u0), x1 = __builtin_bit_cast(u32x2, u1);
        WB[t][s] = u32x4{x0[0], x0[1], x1[0], x1[1]};
      }
      asm volatile("s_waitcnt lgkmcnt(0)" ::: "memory");
    }
  }
  wg_barrier();
  {
    float mw[NSW], lw[NSW];
#pragma unroll
    for (int w = 0; w < NSW; ++w) { mw[w] = lds[L_MRG + w * 132 + 128]; lw[w] = lds[L_MRG + w * 132 + 129]; }
    float M = mw[0];
#pragma unroll
    for (int w = 1; w < NSW; ++w) M = fmaxf(M, mw[w]);
    float L = 0.f, o0 = 0.f, o1 = 0.f;
#pragma unroll
    for (int w = 0; w < NSW; ++w) {
      const float wt = __builtin_amdgcn_exp2f(mw[w] - M);
      const u32x2 ov = *reinterpret_cast<const u32x2*>(&lds[L_MRG + w * 132 + 32 * g + 2 * c]);
      L = fmaf(wt, lw[w], L); o0 = fmaf(wt, __uint_as_float(ov[0]), o0); o1 = fmaf(wt, __uint_as_float(ov[1]), o1);
    }
    const float inv = __builtin_amdgcn_rcpf(L);
    float* ob = &lds[L_H + hw * 64];
    ob[16 * g + c] = __uint_as_float(pk_bf16(o0 * inv, o1 * inv));
    asm volatile("s_waitcnt lgkmcnt(0)" ::: "memory");
    u32x4 oa[4];
#pragma unroll
    for (int s = 0; s < 4; ++s) oa[s] = *reinterpret_cast<const u32x4*>(&ob[16 * s + 4 * g]);
    f32x4 d[8];
#pragma unroll
    for (int t = 0; t < 8; ++t) d[t] = f32x4{0, 0, 0, 0};
#pragma unroll
    for (int s = 0; s < 4; ++s)
#pragma unroll
      for (int t = 0; t < 8; ++t) d[t] = mfma(oa[s], WB[t][s], d[t]);
    unsigned m1 = (g & 1) ? ~0u : 0u, m2 = (g & 2) ? ~0u : 0u;
    asm volatile("" : "+v"(m1), "+v"(m2));
    const float gate = lds[L_SC + 0], yc = lds[L_SC + 1] * lds[L_SC + 2];
    const unsigned tag = __float_as_uint(lds[L_SC + 3]);
    const __amdgpu_buffer_rsrc_t rin = rsrc(p.ws + WS_INBOX + (size_t)b * 32 * 4096, 32 * 4096);
#pragma unroll
    for (int pi = 0; pi < 2; ++pi) {
      const unsigned lo = (m1 & __float_as_uint(d[2 + pi][0])) | (~m1 & __float_as_uint(d[pi][0]));
      const unsigned hi = (m1 & __float_as_uint(d[6 + pi][0])) | (~m1 & __float_as_uint(d[4 + pi][0]));
      const float a0 = __uint_as_float((m2 & hi) | (~m2 & lo));
      const float a1 = dppf<ROR8>(a0);
      if (c < 8) {
        const float v0 = gate * a0 + yc * dy[2 * pi], v1 = gate * a1 + yc * dy[2 * pi + 1];
        const u32x4 gr = {__float_as_uint(v0), tag, __float_as_uint(v1), tag};
        __builtin_amdgcn_raw_buffer_store_b128(gr, rin, (8 * hw + 2 * g + pi) * 4096 + h * 128 + 16 * c, 0, AUX_SC1);
      }
    }
  }
}

extern "C" void kernel_launch(void* const* d_in, const int* in_sizes, int n_in, void* d_out, int out_size, void* d_ws, size_t ws_size,
                              hipStream_t stream) {
  Params p;
  p.xA = (const unsigned short*)d_in[0]; p.K = (const unsigned short*)d_in[1]; p.V = (const unsigned short*)d_in[2];
  p.wq = (const unsigned short*)d_in[3]; p.wo = (const unsigned short*)d_in[4];
  p.wr = (const unsigned short*)d_in[6]; p.wg = (const unsigned short*)d_in[7]; p.wu = (const unsigned short*)d_in[8];
  p.wd = (const unsigned short*)d_in[9]; p.gw = (const unsigned short*)d_in[10];
  p.out = (unsigned short*)d_out; p.ws = (unsigned char*)d_ws;
  fused_decode_kernel<<<NB * NH, NT, 0, stream>>>(p);
}
```

```cpp
#include <hip/hip_runtime.h>
#include <hip/hip_bf16.h>

typedef unsigned u32x4 __attribute__((ext_vector_type(4)));
typedef unsigned u32x2 __attribute__((ext_vector_type(2)));
typedef float f32x4 __attribute__((ext_vector_type(4)));
typedef short s16x4 __attribute__((ext_vector_type(4)));
typedef __bf16 bf16x2 __attribute__((ext_vector_type(2)));
typedef __bf16 bf16x8 __attribute__((ext_vector_type(8)));
#define GAS __attribute__((address_space(1)))
#define LAS __attribute__((address_space(3)))
typedef GAS unsigned gu32;

constexpr int NB = 8, NH = 32, SK = 4096, DH = 128, DM = 512, FF = 1792;
constexpr int NSW = 4;
constexpr int NT = (NSW + 4) * 64;
constexpr int TILES = SK / 16 / NSW;
constexpr int BURST = 3;
constexpr int HD = 8;
constexpr int HD2 = 4;
constexpr unsigned PACE_DT = 117, PACE_T0 = 160, PACE_TH = 100, PACE_M = 100, PACE_Q = 248, T_READ = 3300;
constexpr int PA = 8, PB = 22, PC = 30;
constexpr int CODE_PF = 14848;
constexpr float C2 = 0.08838834764831845f * 1.4426950408889634f;
constexpr int RSRC3 = 0x00020000;
constexpr int AUX_NT = 2, AUX_SC1 = 16;
constexpr int WS_EPOCH = 0, WS_INBOX = 4096, WS_PACE = WS_INBOX + (1 << 20);
constexpr unsigned SPIN_CAP = 1u << 16;

struct Params {
  const unsigned short *K, *V, *xA, *wq, *wo, *wr, *wg, *wu, *wd, *gw;
  unsigned short* out;
  unsigned char* ws;
};

constexpr int L_QP = 0;
constexpr int L_MRG = L_QP + 512;
constexpr int L_GU = L_MRG + NSW * 132;
constexpr int L_H = L_GU + 512;
constexpr int L_SC = L_H + 256;
constexpr int L_PC = L_SC + 8;
constexpr int L_ST = L_PC + 4;
constexpr int L_END = L_ST + 4 * 2048;

__device__ __forceinline__ float dot2(unsigned a, unsigned b, float c) {
  return __builtin_amdgcn_fdot2_f32_bf16(__builtin_bit_cast(bf16x2, a), __builtin_bit_cast(bf16x2, b), c, false);
}
__device__ __forceinline__ unsigned pk_bf16(float lo, float hi) {
  bf16x2 v = {(__bf16)lo, (__bf16)hi};
  return __builtin_bit_cast(unsigned, v);
}
__device__ __forceinline__ float rnd_bf16(float x) { return (float)(__bf16)x; }
__device__ __forceinline__ float bf_lo(unsigned u) { return __builtin_bit_cast(float, u << 16); }
__device__ __forceinline__ float bf_hi(unsigned u) { return __builtin_bit_cast(float, u & 0xffff0000u); }
template <int CTRL> __device__ __forceinline__ float dppf(float x) {
  return __builtin_bit_cast(float, __builtin_amdgcn_mov_dpp(__builtin_bit_cast(int, x), CTRL, 0xf, 0xf, true));
}
template <int CTRL> __device__ __forceinline__ unsigned dppu(unsigned x) {
  return (unsigned)__builtin_amdgcn_mov_dpp((int)x, CTRL, 0xf, 0xf, true);
}
constexpr int XOR1 = 0xB1, XOR2 = 0x4E, XOR7 = 0x141, ROR8 = 0x128, ROR4 = 0x124, BCAST = 0x150;
__device__ __forceinline__ float xrow_sum(float x) {
  auto s = __builtin_amdgcn_permlane16_swap(__float_as_uint(x), __float_as_uint(x), false, false);
  x = __uint_as_float(s[0]) + __uint_as_float(s[1]);
  auto t = __builtin_amdgcn_permlane32_swap(__float_as_uint(x), __float_as_uint(x), false, false);
  return __uint_as_float(t[0]) + __uint_as_float(t[1]);
}
__device__ __forceinline__ float xrow_max(float x) {
  auto s = __builtin_amdgcn_permlane16_swap(__float_as_uint(x), __float_as_uint(x), false, false);
  x = fmaxf(__uint_as_float(s[0]), __uint_as_float(s[1]));
  auto t = __builtin_amdgcn_permlane32_swap(__float_as_uint(x), __float_as_uint(x), false, false);
  return fmaxf(__uint_as_float(t[0]), __uint_as_float(t[1]));
}
__device__ __forceinline__ float wave_sum(float x) {
  x += dppf<XOR1>(x); x += dppf<XOR2>(x); x += dppf<XOR7>(x); x += dppf<ROR8>(x);
  return xrow_sum(x);
}
__device__ __forceinline__ f32x4 mfma(u32x4 a, u32x4 b, f32x4 c) {
  return __builtin_amdgcn_mfma_f32_16x16x32_bf16(__builtin_bit_cast(bf16x8, a), __builtin_bit_cast(bf16x8, b), c, 0, 0, 0);
}
__device__ __forceinline__ void wg_barrier() {
  asm volatile("s_waitcnt lgkmcnt(0)" ::: "memory");
  __builtin_amdgcn_s_barrier();
  asm volatile("" ::: "memory");
}
__device__ __forceinline__ float sigmoidf_(float x) { return 1.f / (1.f + __expf(-x)); }
__device__ __forceinline__ float safe_max(float m) { return m == -INFINITY ? 0.f : m; }
__device__ __forceinline__ __amdgpu_buffer_rsrc_t rsrc(const void* p, int bytes) {
  return __builtin_amdgcn_make_buffer_rsrc(const_cast<void*>(p), 0, bytes, RSRC3);
}
__device__ __forceinline__ f32x4 vm_step(u32x4 w, unsigned pp, const u32x4& msk, f32x4 d) {
  u32x4 bq = {pp & msk[0], pp & msk[1], pp & msk[2], pp & msk[3]};
  return mfma(w, bq, d);
}
__device__ __forceinline__ unsigned rep16(unsigned pair, int odd) {
  return odd ? __builtin_amdgcn_perm(pair, pair, 0x03020302u) : __builtin_amdgcn_perm(pair, pair, 0x01000100u);
}
__device__ __forceinline__ u32x4 ldw(const unsigned short* p) { return __builtin_nontemporal_load(reinterpret_cast<const u32x4*>(p)); }
__device__ __forceinline__ unsigned lds_word(const float* p) { return __hip_atomic_load((LAS unsigned*)p, __ATOMIC_RELAXED, __HIP_MEMORY_SCOPE_WORKGROUP); }
#define PIN() __builtin_amdgcn_sched_barrier(0)

__global__ void __launch_bounds__(NT, NT / 256) fused_decode_kernel(Params p) {
  __shared__ __attribute__((aligned(16))) float lds[L_END];
  const int tid = threadIdx.x, lane = tid & 63, wave = __builtin_amdgcn_readfirstlane(tid >> 6);
  const int g = lane >> 4, c = lane & 15;
  const int bid = blockIdx.x, h = (bid & 7) * 4 + ((bid >> 3) & 3), b = bid >> 5;
  const unsigned long long t_entry = __builtin_amdgcn_s_memrealtime();
  unsigned long long pc0;
  asm volatile("s_getpc_b64 %0" : "=s"(pc0));
  const unsigned short *Kp = p.K, *Vp = p.V;
  asm volatile("" : "+s"(Kp), "+s"(Vp));

  if (__builtin_expect(wave < NSW, 1)) {
    const size_t bh = (size_t)(b * NH + h) * SK * DH;
    const __amdgpu_buffer_rsrc_t rk = rsrc(Kp + bh, SK * DH * 2), rv = rsrc(Vp + bh, SK * DH * 2);
    const int voff = lane * 16;
    unsigned cpf[2] = {0u, 0u};
    static_assert(CODE_PF <= 16384, "two loads; CODE_PF must not exceed the length of the kernel's code");
    if (wave == NSW - 1) {
      const unsigned char* pc = (const unsigned char*)(pc0 & ~127ull);
#pragma unroll
      for (int i = 0; i < (CODE_PF + 8191) / 8192; ++i)
        if (i * 8192 + lane * 128 < CODE_PF) cpf[i] = *(const GAS unsigned*)(pc + i * 8192 + lane * 128);
    }
    auto ldk = [&](int i, int j) { return __builtin_amdgcn_raw_buffer_load_b128(rk, voff, (i * NSW + wave) * 4096 + j * 1024, AUX_NT); };
    auto ldv = [&](int i, int j) { return __builtin_amdgcn_raw_buffer_load_b128(rv, voff, (i * NSW + wave) * 4096 + j * 1024, AUX_NT); };
    u32x4 RK[4], RV[4], EK[BURST][4], EV[BURST][4];
#pragma unroll
    for (int j = 0; j < 4; ++j) RK[j] = ldk(BURST, j);
#pragma unroll
    for (int j = 0; j < 4; ++j) RV[j] = ldv(BURST, j);
#pragma unroll
    for (int e = 0; e < BURST; ++e) {
#pragma unroll
      for (int j = 0; j < 4; ++j) EK[e][j] = ldk(e, j);
#pragma unroll
      for (int j = 0; j < 4; ++j) EV[e][j] = ldv(e, j);
    }
    wg_barrier();
    asm volatile("" :: "v"(cpf[0]), "v"(cpf[1]));
    unsigned qp[4];
    {
      f32x4 q0 = {0, 0, 0, 0}, q1 = {0, 0, 0, 0};
#pragma unroll
      for (int w = 0; w < 4; ++w) {
        q0 += *reinterpret_cast<const f32x4*>(&lds[L_QP + w * 128 + 8 * c]);
        q1 += *reinterpret_cast<const f32x4*>(&lds[L_QP + w * 128 + 8 * c + 4]);
      }
      qp[0] = pk_bf16(q0[0], q0[1]); qp[1] = pk_bf16(q0[2], q0[3]); qp[2] = pk_bf16(q1[0], q1[1]); qp[3] = pk_bf16(q1[2], q1[3]);
    }
    float m = -INFINITY, ms = 0.f, l = 0.f, acc[8];
#pragma unroll
    for (int k = 0; k < 8; ++k) acc[k] = 0.f;
    const bool b8 = c & 8, b4 = c & 4;

    auto process = [&](u32x4 (&TK)[4], u32x4 (&TV)[4], bool refill, int inext) {
      float x[4];
#pragma unroll
      for (int j = 0; j < 4; ++j) {
        float d = dot2(TK[j][0], qp[0], 0.f);
        d = dot2(TK[j][1], qp[1], d); d = dot2(TK[j][2], qp[2], d); d = dot2(TK[j][3], qp[3], d);
        x[j] = d;
        if (refill) { TK[j] = ldk(inext, j); PIN(); }
      }
      const float a0 = x[0] + dppf<ROR8>(x[0]), a1 = x[1] + dppf<ROR8>(x[1]), a2 = x[2] + dppf<ROR8>(x[2]), a3 = x[3] + dppf<ROR8>(x[3]);
      const float y0 = b8 ? a2 : a0, y1 = b8 ? a3 : a1;
      const float z0 = y0 + dppf<XOR7>(y0), z1 = y1 + dppf<XOR7>(y1);
      float z = b4 ? z1 : z0;
      z += dppf<XOR2>(z); z += dppf<XOR1>(z);
      const float sc = z * C2;
      float pm = fmaxf(sc, dppf<ROR4>(sc)); pm = fmaxf(pm, dppf<ROR8>(pm));
      if (__any(pm > m)) {
        const float mn = fmaxf(m, pm), msn = safe_max(mn), al = __builtin_amdgcn_exp2f(m == -INFINITY ? -INFINITY : m - msn);
        l *= al;
#pragma unroll
        for (int k = 0; k < 8; ++k) acc[k] *= al;
        m = mn; ms = msn;
      }
      const float pe = __builtin_amdgcn_exp2f(sc - ms);
      l += pe;
      const unsigned plo = pk_bf16(pe, 0.f), phi = plo << 16;
#define PV(j) { const unsigned lo_ = dppu<BCAST + 4 * (j)>(plo), hi_ = dppu<BCAST + 4 * (j)>(phi); \
        _Pragma("unroll") for (int k = 0; k < 4; ++k) { acc[2 * k] = dot2(TV[j][k], lo_, acc[2 * k]); acc[2 * k + 1] = dot2(TV[j][k], hi_, acc[2 * k + 1]); } \
        if (refill) { TV[j] = ldv(inext, j); PIN(); } }
      PV(0) PV(1) PV(2) PV(3)
#undef PV
      asm volatile("" : "+v"(acc[0]), "+v"(acc[1]), "+v"(acc[2]), "+v"(acc[3]), "+v"(acc[4]), "+v"(acc[5]), "+v"(acc[6]), "+v"(acc[7]));
    };
    process(RK, RV, true, BURST + 1);
#pragma unroll
    for (int e = 0; e < BURST; ++e) process(EK[e], EV[e], false, 0);
    unsigned long long tnext = t_entry + PACE_T0 + BURST * PACE_DT;
    unsigned dt = PACE_DT;
    for (int i = BURST + 1; i < TILES - 1; ++i) {
      tnext += dt;
      unsigned long long now = __builtin_amdgcn_s_memrealtime();
      while ((long long)(now - tnext) < 0) { __builtin_amdgcn_s_sleep(1); now = __builtin_amdgcn_s_memrealtime(); }
      if (wave == 0 && (i == PA || i == PB)) { if (lane == 0) __hip_atomic_store((LAS unsigned*)&lds[L_PC + (i == PB)], (unsigned)(now - t_entry), __ATOMIC_RELAXED, __HIP_MEMORY_SCOPE_WORKGROUP); }
      if (i == PC) {
        const unsigned nl = __builtin_amdgcn_readfirstlane(lds_word(&lds[L_PC + 2]));
        if (nl != 0u) { tnext = t_entry + (nl & 0xffffu); dt = nl >> 16; }
      }
      process(RK, RV, true, i + 1);
    }
    process(RK, RV, false, 0);
    unsigned xfin = 0;
    if (wave == 0 && lane < 16) xfin = p.xA[b * DM + 16 * h + lane];
    {
      const float M = xrow_max(m), sc = __builtin_amdgcn_exp2f(m - safe_max(M));
      float ls = l * sc;
      ls += dppf<ROR4>(ls); ls += dppf<ROR8>(ls);
      ls = xrow_sum(ls);
      float o[8];
#pragma unroll
      for (int k = 0; k < 8; ++k) o[k] = xrow_sum(acc[k] * sc);
      float* rec = &lds[L_MRG + wave * 132];
      if (g == 0) {
        *reinterpret_cast<f32x4*>(rec + 8 * c) = f32x4{o[0], o[1], o[2], o[3]};
        *reinterpret_cast<f32x4*>(rec + 8 * c + 4) = f32x4{o[4], o[5], o[6], o[7]};
        if (c == 0) { rec[128] = M; rec[129] = ls; }
      }
    }
    wg_barrier();
    if (wave != 0) return;
    const unsigned tag = __float_as_uint(lds[L_SC + 3]);
    const __amdgpu_buffer_rsrc_t rin = rsrc(p.ws + WS_INBOX + (size_t)(b * 32 + h) * 4096, 4096);
    u32x4 gv[4];
#pragma unroll
    for (int q = 0; q < 4; ++q) gv[q] = u32x4{0u, ~tag, 0u, ~tag};
    for (unsigned spins = 0;; ++spins) {
#pragma unroll
      for (int q = 0; q < 4; ++q)
        if (gv[q][1] != tag || gv[q][3] != tag) gv[q] = __builtin_amdgcn_raw_buffer_load_b128(rin, lane * 16 + q * 1024, 0, AUX_SC1);
      int missing = 0;
#pragma unroll
      for (int q = 0; q < 4; ++q) missing += __popcll(__ballot(gv[q][1] != tag || gv[q][3] != tag));
      if (missing == 0 || spins > SPIN_CAP) break;
      if (missing > 192) __builtin_amdgcn_s_sleep(8);
    }
    float s0 = 0.f, s1 = 0.f;
#pragma unroll
    for (int q = 0; q < 4; ++q) { s0 += __uint_as_float(gv[q][0]); s1 += __uint_as_float(gv[q][2]); }
    s0 += dppf<ROR8>(s0); s1 += dppf<ROR8>(s1);
    s0 = xrow_sum(s0); s1 = xrow_sum(s1);
    if (lane < 16) {
      const float s = lane < 8 ? s0 : s1;
      const float xb = __builtin_bit_cast(float, xfin << 16);
      const float r = s + lds[L_SC + 1] * (xb + xb * 0.f);
      p.out[b * DM + 16 * h + lane] = __builtin_bit_cast(unsigned short, (__bf16)r);
    }
    if (h == 0 && lane == 0) __hip_atomic_store((gu32*)(p.ws + WS_EPOCH + b * 256), tag, __ATOMIC_RELAXED, __HIP_MEMORY_SCOPE_AGENT);
    return;
  }

  const int hw = wave - NSW;
  __builtin_amdgcn_s_setprio(3);
  u32x4 msk;
#pragma unroll
  for (int w = 0; w < 4; ++w) msk[w] = (c < 8 && (c >> 1) == w) ? ((c & 1) ? 0xffff0000u : 0x0000ffffu) : 0u;
  u32x4 W[16], xp[4];
  {
    const __amdgpu_buffer_rsrc_t rq = rsrc(p.wq, DM * NH * DH * 2);
    const u32x4* xs = reinterpret_cast<const u32x4*>(p.xA + b * DM + 128 * hw + 32 * g);
#pragma unroll
    for (int i = 0; i < 4; ++i) xp[i] = xs[i];
    const int voff = (128 * hw + 32 * g) * 8192 + h * 256 + 16 * c;
#pragma unroll
    for (int i = 0; i < HD; ++i) W[i] = __builtin_amdgcn_raw_buffer_load_b128(rq, voff, i * 8192, AUX_NT);
    if (hw == 0 && lane == 0) lds[L_SC + 4] = 0.f;
    if (hw == 0 && lane < 4) lds[L_PC + lane] = 0.f;
    f32x4 d = {0, 0, 0, 0};
#pragma unroll
    for (int i = 0; i < 32; ++i) {
      d = vm_step(W[i % HD], rep16(xp[i >> 3][(i >> 1) & 3], i & 1), msk, d);
      if (i + HD < 32) W[i % HD] = __builtin_amdgcn_raw_buffer_load_b128(rq, voff, (i + HD) * 8192, AUX_NT);
      PIN();
    }
    if (c < 8) {
#pragma unroll
      for (int r = 0; r < 4; ++r) lds[L_QP + hw * 128 + 32 * g + 8 * r + c] = d[r];
    }
  }
  __builtin_amdgcn_s_setprio(0);
  wg_barrier();
  if (hw == 2 && lane == 0) {
    const unsigned epoch = __hip_atomic_load((gu32*)(p.ws + WS_EPOCH + b * 256), __ATOMIC_RELAXED, __HIP_MEMORY_SCOPE_AGENT);
    lds[L_SC + 3] = __uint_as_float(epoch + 1u);
  }
  if (hw == 3) {
    const u32x4 xa = *reinterpret_cast<const u32x4*>(p.xA + b * DM + 8 * lane), gw = *reinterpret_cast<const u32x4*>(p.gw + 8 * lane);
    const u32x4 wr = *reinterpret_cast<const u32x4*>(p.wr + 8 * lane);
    float sg = 0.f, sr = 0.f;
#pragma unroll
    for (int k = 0; k < 4; ++k) {
      sg += rnd_bf16(bf_lo(xa[k]) * bf_lo(gw[k])) + rnd_bf16(bf_hi(xa[k]) * bf_hi(gw[k]));
      sr = dot2(xa[k], wr[k], sr);
    }
    sg = wave_sum(sg); sr = wave_sum(sr);
    const float gate = rnd_bf16(sigmoidf_(rnd_bf16(sg)));
    const float rs = rnd_bf16(sigmoidf_(rnd_bf16(sr)));
    if (lane == 0) { lds[L_SC + 0] = gate; lds[L_SC + 1] = rnd_bf16(1.f - gate); lds[L_SC + 2] = rs; }
  }
  f32x4 dy = {0, 0, 0, 0};
  const int hs = (h == 7 || h == 15 || h == 23) ? NH : (h >= 28 && h <= 30) ? 8 * (h - 28) + 7 : h;
  if (hs < FF / 64) {
    {
      f32x4 d = {0, 0, 0, 0};
      const unsigned short* wbase = (c < 8 ? p.wg : p.wu) + 64 * hs + 8 * (c & 7) + (size_t)(128 * hw + 32 * g) * FF;
#pragma unroll
      for (int i = 0; i < HD2; ++i) W[i] = ldw(wbase + (size_t)i * FF);
#pragma unroll
      for (int i = 0; i < 32; ++i) {
        d = vm_step(W[i % HD2], rep16(xp[i >> 3][(i >> 1) & 3], i & 1), msk, d);
        if (i + HD2 < 32) W[i % HD2] = ldw(wbase + (size_t)(i + HD2) * FF);
        PIN();
      }
      if (c < 8) {
#pragma unroll
        for (int r = 0; r < 4; ++r) lds[L_GU + hw * 128 + 32 * g + 8 * r + c] = d[r];
      }
      asm volatile("s_waitcnt lgkmcnt(0)" ::: "memory");
      if (lane == 0) __hip_atomic_fetch_add((LAS unsigned*)&lds[L_SC + 4], 1u, __ATOMIC_RELAXED, __HIP_MEMORY_SCOPE_WORKGROUP);
    }
    const __amdgpu_buffer_rsrc_t rd_ = rsrc(p.wd, FF * DM * 2);
    const int voff = (64 * hs + 16 * g) * 1024 + 256 * hw + 16 * c;
#pragma unroll
    for (int i = 0; i < 16; ++i) {
      W[i] = __builtin_amdgcn_raw_buffer_load_b128(rd_, voff, i * 1024, AUX_NT);
      if ((i % HD2) == HD2 - 1) asm volatile("s_waitcnt vmcnt(0)" ::: "memory");
    }
    while (lds_word(&lds[L_SC + 4]) < 4u) __builtin_amdgcn_s_sleep(4);
    asm volatile("" ::: "memory");
    float gt = 0.f, up = 0.f;
#pragma unroll
    for (int w = 0; w < 4; ++w) { gt += lds[L_GU + w * 128 + lane]; up += lds[L_GU + w * 128 + 64 + lane]; }
    gt = rnd_bf16(gt); up = rnd_bf16(up);
    const float si = rnd_bf16(gt * sigmoidf_(gt));
    lds[L_H + hw * 64 + lane] = __uint_as_float(pk_bf16(si * up, si * up));
    asm volatile("s_waitcnt lgkmcnt(0)" ::: "memory");
    u32x4 hp[4];
#pragma unroll
    for (int i = 0; i < 4; ++i) hp[i] = *reinterpret_cast<const u32x4*>(&lds[L_H + hw * 64 + 16 * g + 4 * i]);
#pragma unroll
    for (int i = 0; i < 16; ++i) dy = vm_step(W[i], hp[i >> 2][i & 3], msk, dy);
  }
  if (hw == 2) {
    while (lds_word(&lds[L_PC + 1]) == 0u && __builtin_amdgcn_s_memrealtime() - t_entry < T_READ) __builtin_amdgcn_s_sleep(16);
    asm volatile("" ::: "memory");
    const unsigned tag = __float_as_uint(lds[L_SC + 3]);
    const unsigned ta = lds_word(&lds[L_PC + 0]), tb = lds_word(&lds[L_PC + 1]);
    if (lane == 0 && tb != 0u)
      __hip_atomic_store((GAS unsigned long long*)(p.ws + WS_PACE + b * 256 + h * 8), ((unsigned long long)tag << 32) | (tb << 16) | ta, __ATOMIC_RELAXED, __HIP_MEMORY_SCOPE_AGENT);
    while (__builtin_amdgcn_s_memrealtime() - t_entry < T_READ) __builtin_amdgcn_s_sleep(16);
    unsigned long long rec = 0;
    if (lane < 32) rec = __hip_atomic_load((GAS unsigned long long*)(p.ws + WS_PACE + b * 256 + lane * 8), __ATOMIC_RELAXED, __HIP_MEMORY_SCOPE_AGENT);
    const bool ok = lane < 32 && (unsigned)(rec >> 32) == tag;
    const unsigned n = __popcll(__ballot(ok));
    unsigned sa = __builtin_amdgcn_readfirstlane((unsigned)wave_sum(ok ? (float)((unsigned)rec & 0xffffu) : 0.f));
    unsigned sb = __builtin_amdgcn_readfirstlane((unsigned)wave_sum(ok ? (float)((unsigned)rec >> 16) : 0.f));
    if (n >= 16u) {
      sa /= n; sb /= n;
      const unsigned slope = ((sb - sa) * PACE_Q) / (256u * (PB - PA));
      const unsigned at_pc = sb + ((sb - sa) * (PC - PB)) / (PB - PA);
      const unsigned line_pc = PACE_T0 + PC * PACE_DT, line_end = PACE_T0 + (TILES - 2) * PACE_DT;
      const unsigned end = at_pc + slope * (TILES - 2 - PC);
      if (end > line_end + PACE_TH) {
        const unsigned base = min(max(at_pc - PACE_M, line_pc), line_pc + 2000u);
        const unsigned nslope = (end - PACE_M - min(base, end - PACE_M) + (TILES - 2 - PC) / 2) / (TILES - 2 - PC);
        if (lane == 0) __hip_atomic_store((LAS unsigned*)&lds[L_PC + 2], base | (min(max(nslope, 100u), 200u) << 16), __ATOMIC_RELAXED, __HIP_MEMORY_SCOPE_WORKGROUP);
      }
    }
  }
  u32x4 WB[8][4];
  {
    const __amdgpu_buffer_rsrc_t ro = rsrc(p.wo, NH * DH * DM * 2);
    const int voff = (128 * h + g) * 1024 + 256 * hw + 16 * c;
    float* st = &lds[L_ST + hw * 2048];
#pragma unroll
    for (int s = 0; s < 4; ++s) {
#pragma unroll
      for (int hf = 0; hf < 2; ++hf) {
#pragma unroll
        for (int i = 0; i < 4; ++i) W[i] = __builtin_amdgcn_raw_buffer_load_b128(ro, voff, (32 * s + 16 * hf + 4 * i) * 1024, AUX_NT);
#pragma unroll
        for (int i = 0; i < 4; ++i) *reinterpret_cast<u32x4*>(&st[(16 * hf + 4 * i + g) * 64 + 4 * c]) = W[i];
      }
      asm volatile("s_waitcnt lgkmcnt(0)" ::: "memory");
#pragma unroll
      for (int t = 0; t < 8; ++t) {
        const float* a0 = &st[(8 * g + (c >> 2)) * 64 + 8 * t + 2 * (c & 3)];
        const s16x4 u0 = __builtin_amdgcn_ds_read_tr16_b64_v4i16((LAS s16x4*)a0);
        const s16x4 u1 = __builtin_amdgcn_ds_read_tr16_b64_v4i16((LAS s16x4*)(a0 + 4 * 64));
        const u32x2 x0 = __builtin_bit_cast(u32x2, u0), x1 = __builtin_bit_cast(u32x2, u1);
        WB[t][s] = u32x4{x0[0], x0[1], x1[0], x1[1]};
      }
      asm volatile("s_waitcnt lgkmcnt(0)" ::: "memory");
    }
  }
  wg_barrier();
  {
    float mw[NSW], lw[NSW];
#pragma unroll
    for (int w = 0; w < NSW; ++w) { mw[w] = lds[L_MRG + w * 132 + 128]; lw[w] = lds[L_MRG + w * 132 + 129]; }
    float M = mw[0];
#pragma unroll
    for (int w = 1; w < NSW; ++w) M = fmaxf(M, mw[w]);
    float L = 0.f, o0 = 0.f, o1 = 0.f;
#pragma unroll
    for (int w = 0; w < NSW; ++w) {
      const float wt = __builtin_amdgcn_exp2f(mw[w] - M);
      const u32x2 ov = *reinterpret_cast<const u32x2*>(&lds[L_MRG + w * 132 + 32 * g + 2 * c]);
      L = fmaf(wt, lw[w], L); o0 = fmaf(wt, __uint_as_float(ov[0]), o0); o1 = fmaf(wt, __uint_as_float(ov[1]), o1);
    }
    const float inv = __builtin_amdgcn_rcpf(L);
    float* ob = &lds[L_H + hw * 64];
    ob[16 * g + c] = __uint_as_float(pk_bf16(o0 * inv, o1 * inv));
    asm volatile("s_waitcnt lgkmcnt(0)" ::: "memory");
    u32x4 oa[4];
#pragma unroll
    for (int s = 0; s < 4; ++s) oa[s] = *reinterpret_cast<const u32x4*>(&ob[16 * s + 4 * g]);
    f32x4 d[8];
#pragma unroll
    for (int t = 0; t < 8; ++t) d[t] = f32x4{0, 0, 0, 0};
#pragma unroll
    for (int s = 0; s < 4; ++s)
#pragma unroll
      for (int t = 0; t < 8; ++t) d[t] = mfma(oa[s], WB[t][s], d[t]);
    unsigned m1 = (g & 1) ? ~0u : 0u, m2 = (g & 2) ? ~0u : 0u;
    asm volatile("" : "+v"(m1), "+v"(m2));
    const float gate = lds[L_SC + 0], yc = lds[L_SC + 1] * lds[L_SC + 2];
    const unsigned tag = __float_as_uint(lds[L_SC + 3]);
    const __amdgpu_buffer_rsrc_t rin = rsrc(p.ws + WS_INBOX + (size_t)b * 32 * 4096, 32 * 4096);
#pragma unroll
    for (int pi = 0; pi < 2; ++pi) {
      const unsigned lo = (m1 & __float_as_uint(d[2 + pi][0])) | (~m1 & __float_as_uint(d[pi][0]));
      const unsigned hi = (m1 & __float_as_uint(d[6 + pi][0])) | (~m1 & __float_as_uint(d[4 + pi][0]));
      const float a0 = __uint_as_float((m2 & hi) | (~m2 & lo));
      const float a1 = dppf<ROR8>(a0);
      if (c < 8) {
        const float v0 = gate * a0 + yc * dy[2 * pi], v1 = gate * a1 + yc * dy[2 * pi + 1];
        const u32x4 gr = {__float_as_uint(v0), tag, __float_as_uint(v1), tag};
        __builtin_amdgcn_raw_buffer_store_b128(gr, rin, (8 * hw + 2 * g + pi) * 4096 + h * 128 + 16 * c, 0, AUX_SC1);
      }
    }
  }
}

extern "C" void kernel_launch(void* const* d_in, const int* in_sizes, int n_in, void* d_out, int out_size, void* d_ws, size_t ws_size,
                              hipStream_t stream) {
  Params p;
  p.xA = (const unsigned short*)d_in[0]; p.K = (const unsigned short*)d_in[1]; p.V = (const unsigned short*)d_in[2];
  p.wq = (const unsigned short*)d_in[3]; p.wo = (const unsigned short*)d_in[4];
  p.wr = (const unsigned short*)d_in[6]; p.wg = (const unsigned short*)d_in[7]; p.wu = (const unsigned short*)d_in[8];
  p.wd = (const unsigned short*)d_in[9]; p.gw = (const unsigned short*)d_in[10];
  p.out = (unsigned short*)d_out; p.ws = (unsigned char*)d_ws;
  fused_decode_kernel<<<NB * NH, NT, 0, stream>>>(p);
}
```
